```python
import torch

def _softmax(x, axis=-1):
    return torch.softmax(x, dim=axis)
def _gelu(x):
    return torch.nn.functional.gelu(x, approximate="tanh")
def _silu(x):
    return torch.nn.functional.silu(x)
def _sigmoid(x):
    return torch.sigmoid(x)
def _layernorm(x, axis=-1, eps=1e-5):
    m = x.mean(dim=axis, keepdim=True)
    v = x.var(dim=axis, keepdim=True, unbiased=False)
    return (x - m) / torch.sqrt(v + eps)
def _scatter_add(tgt, idx, src, axis=0):
    return tgt.clone().index_add_(axis, idx, src)
def _segment_sum(src, seg, n):
    out = torch.zeros((n,) + src.shape[1:], dtype=src.dtype)
    return out.index_add_(0, seg, src)
def _pad(x, pw):
    flat = tuple(v for pair in reversed(pw) for v in pair)
    return torch.nn.functional.pad(x, flat)

kernel_name = 'standalone__gqa_softmax__near_ridge'

def setup_inputs():

    _w = (lambda a: (lambda t: t.to(torch.bfloat16) if t.is_floating_point() else t)(torch.as_tensor(a)))
    B, Hq, Hkv, Sq, Sk, D = 16, 32, 8, 64, 512, 128
    return {
        "q": _w((lambda *s: torch.randn(*s, dtype=torch.bfloat16) * 0.8)(B, Hq, Sq, D)),
        "k": _w((lambda *s: torch.randn(*s, dtype=torch.bfloat16) * 0.8)(B, Hkv, Sk, D)),
        "v": _w((lambda *s: torch.randn(*s, dtype=torch.bfloat16) * 0.05)(B, Hkv, Sk, D)),
    }

def reference(q, k, v):
    B, Hq, Sq, D = q.shape
    Hkv = k.shape[1]
    rep = Hq // Hkv
    k = (lambda x, n, axis: torch.repeat_interleave(x, n, dim=axis))(k, rep, axis=1)
    v = (lambda x, n, axis: torch.repeat_interleave(x, n, dim=axis))(v, rep, axis=1)
    scale = D ** -0.5
    scores = torch.einsum("bhqd,bhkd->bhqk", q, k) * scale
    p = _softmax(scores, axis=-1)
    return torch.einsum("bhqk,bhkd->bhqd", p, v)
```

```cpp
#include <hip/hip_runtime.h>
#include <hip/hip_bf16.h>

typedef __bf16 bf16x8 __attribute__((ext_vector_type(8)));
typedef short s16x4 __attribute__((ext_vector_type(4)));
typedef short s16x8 __attribute__((ext_vector_type(8)));
typedef float f32x16 __attribute__((ext_vector_type(16)));
typedef float f32x2 __attribute__((ext_vector_type(2)));
typedef __bf16 bf16x2 __attribute__((ext_vector_type(2)));
typedef unsigned u32x4 __attribute__((ext_vector_type(4)));
typedef __attribute__((address_space(3))) const char* lds_cptr;

constexpr int LDS_K = 0, LDS_V = 65536, LDS_Q = 131072, LDS_BYTES = 163840;
constexpr float C2 = 0.12751743082459868f;
constexpr float THR = 8.0f;

#define DI __device__ __forceinline__
#define SBAR() __builtin_amdgcn_sched_barrier(0)
#define PIN(x) asm volatile("" : "+v"(x))
#define MFMA(a, b, c) __builtin_amdgcn_mfma_f32_32x32x16_bf16(a, b, c, 0, 0, 0)
#define WAIT_BAR(N) asm volatile("s_waitcnt vmcnt(" #N ") lgkmcnt(0)\n\ts_barrier" ::: "memory")
#define MX3(a, b, c) __builtin_fmaxf(__builtin_fmaxf((a), (b)), (c))

DI unsigned cvtpk(float lo, float hi) { f32x2 v = {lo, hi}; bf16x2 b = __builtin_convertvector(v, bf16x2); return __builtin_bit_cast(unsigned, b); }
DI unsigned rfl(unsigned x) { return __builtin_amdgcn_readfirstlane(x); }

DI void dma16(unsigned m0v, unsigned voff, __amdgpu_buffer_rsrc_t srd, unsigned soff) {
  asm volatile("s_mov_b32 m0, %0\n\ts_nop 0\n\tbuffer_load_dwordx4 %1, %2, %3 offen lds" :: "s"(m0v), "v"(voff), "s"(srd), "s"(soff) : "memory");
}
DI bf16x8 kread(lds_cptr p) { return *(const __attribute__((address_space(3))) bf16x8*)p; }
DI s16x4 vtr(lds_cptr p) { return __builtin_amdgcn_ds_read_tr16_b64_v4i16((__attribute__((address_space(3))) s16x4*)p); }

DI float rowmax(const f32x16& p0, const f32x16& p1) {
  float a = MX3(p0[0], p0[1], p1[0]), b = MX3(p0[2], p0[3], p1[1]); a = MX3(a, p1[2], p1[3]);
#pragma unroll
  for (int r = 4; r < 16; r += 4) { a = MX3(a, p0[r], p0[r + 1]); b = MX3(b, p0[r + 2], p0[r + 3]); a = MX3(a, p1[r], p1[r + 1]); b = MX3(b, p1[r + 2], p1[r + 3]); }
  float m = __builtin_fmaxf(a, b);
  auto rr = __builtin_amdgcn_permlane32_swap(__float_as_uint(m), __float_as_uint(m), false, false);
  return __builtin_fmaxf(__uint_as_float(rr[0]), __uint_as_float(rr[1]));
}

__global__ __launch_bounds__(256, 1) void gqa_fwd(const unsigned short* __restrict__ Q, const unsigned short* __restrict__ K,
                                                  const unsigned short* __restrict__ V, unsigned short* __restrict__ O) {
  __shared__ __attribute__((aligned(16))) char lds[LDS_BYTES];
  const unsigned tid = threadIdx.x, lane = tid & 63u, r32 = lane & 31u, hi = lane >> 5;
  const unsigned wid = rfl(tid >> 6);
  const unsigned bid = blockIdx.x, pair = (bid & 7u) * 16u + (bid >> 4), half = (bid >> 3) & 1u;
  const unsigned lds0 = (unsigned)(size_t)(lds_cptr)lds;

  const __amdgpu_buffer_rsrc_t srdK = __builtin_amdgcn_make_buffer_rsrc((void*)(K + (size_t)pair * 65536), (short)0, 131072, 0x00020000);
  const __amdgpu_buffer_rsrc_t srdV = __builtin_amdgcn_make_buffer_rsrc((void*)(V + (size_t)pair * 65536), (short)0, 131072, 0x00020000);
  const __amdgpu_buffer_rsrc_t srdQ = __builtin_amdgcn_make_buffer_rsrc((void*)(Q + (size_t)(pair * 2 + half) * 16384), (short)0, 32768, 0x00020000);

  const unsigned rg = (lane >> 2) & 7u, bb = (lane >> 4) & 1u, sl = lane & 3u;
  const unsigned voffV = rg * 256u + hi * 64u + sl * 16u;
  const unsigned voffK0 = rg * 256u + hi * 64u + ((sl ^ bb) * 16u);
  const unsigned voffK1 = rg * 256u + hi * 64u + ((sl ^ (2u + bb)) * 16u);
  const unsigned wK = lds0 + LDS_K + wid * 4096u, wV = lds0 + LDS_V + wid * 4096u, wS = wid * 4096u;

#define DMA_K(t) do { const unsigned m_ = wK + ((t) & 3) * 16384u, s_ = wS + (t) * 16384u; \
    dma16(m_, voffK0, srdK, s_); dma16(m_ + 1024u, voffK0, srdK, s_ + 128u); dma16(m_ + 2048u, voffK1, srdK, s_ + 2048u); dma16(m_ + 3072u, voffK1, srdK, s_ + 2176u); } while (0)
#define DMA_V(t) do { const unsigned m_ = wV + ((t) & 3) * 16384u, s_ = wS + (t) * 16384u; \
    dma16(m_, voffV, srdV, s_); dma16(m_ + 1024u, voffV, srdV, s_ + 128u); dma16(m_ + 2048u, voffV, srdV, s_ + 2048u); dma16(m_ + 3072u, voffV, srdV, s_ + 2176u); } while (0)

  { const unsigned m_ = lds0 + LDS_Q + wid * 8192u, s_ = wid * 8192u;
#pragma unroll
    for (int p = 0; p < 8; ++p) dma16(m_ + p * 1024u, (p & 2) ? voffK1 : voffK0, srdQ, s_ + (p >> 1) * 2048u + (p & 1) * 128u); }
  DMA_K(0); DMA_K(1); DMA_V(0); DMA_K(2); DMA_V(1); DMA_K(3); DMA_V(2); DMA_V(3);

  const unsigned kb_lane = 2048u * (r32 >> 3) + 64u * (r32 & 7u);
  const lds_cptr kp0 = (lds_cptr)lds + kb_lane + 16u * ((0u + hi) ^ ((r32 >> 2) & 3u));
  const lds_cptr kp1 = (lds_cptr)lds + kb_lane + 16u * ((2u + hi) ^ ((r32 >> 2) & 3u));
  const lds_cptr vp = (lds_cptr)lds + LDS_V + ((lane & 3u) << 3) + (((lane >> 2) & 3u) << 6) + (((lane >> 4) & 1u) << 5) + (hi << 8);

  bf16x8 qr[8], kf[16];
  s16x4 vlo[16], vhi[16];
  u32x4 pw0, pw1, pw2, pw3;
  f32x16 o0 = {}, o1 = {}, o2 = {}, o3 = {}, pA0, pA1, pB0, pB1;
  float mhat, l_reg = 0.f;
  const f32x16 zero16 = {};

  asm volatile("s_waitcnt vmcnt(32)" ::: "memory");
#pragma unroll
  for (int d0 = 0; d0 < 8; ++d0) qr[d0] = kread(((d0 & 1) ? kp1 : kp0) + LDS_Q + wid * 8192u + 512 * (d0 >> 1));

#define KRD(i, t) kf[i] = kread(((((i) >> 1) & 1) ? kp1 : kp0) + ((t) & 3) * 16384 + ((i) & 1) * 8192 + 512 * ((i) >> 2))
#define VRD(i, t) do { vlo[i] = vtr(vp + ((t) & 3) * 16384 + 4096 * ((i) >> 2) + 512 * ((i) & 3)); vhi[i] = vtr(vp + ((t) & 3) * 16384 + 4096 * ((i) >> 2) + 512 * ((i) & 3) + 2048); } while (0)
#define VFR(i) __builtin_bit_cast(bf16x8, __builtin_shufflevector(vlo[i], vhi[i], 0, 1, 2, 3, 4, 5, 6, 7))
#define PAF(k) __builtin_bit_cast(bf16x8, pw##k)
#define EX(v) __builtin_amdgcn_exp2f(__builtin_fmaf((v), C2, nmh))
#define PKW(P, i) cvtpk(P[i], P[i + 1])

  WAIT_BAR(28);
#pragma unroll
  for (int i = 0; i < 16; ++i) KRD(i, 0);
  pA0 = MFMA(kf[0], qr[0], zero16); pA1 = MFMA(kf[1], qr[0], zero16);
#pragma unroll
  for (int d0 = 1; d0 < 8; ++d0) { pA0 = MFMA(kf[2 * d0], qr[d0], pA0); pA1 = MFMA(kf[2 * d0 + 1], qr[d0], pA1); }
  WAIT_BAR(24);
  DMA_K(4);
#pragma unroll
  for (int i = 0; i < 16; ++i) KRD(i, 1);
  { const float rm = rowmax(pA0, pA1); mhat = rm * C2; const float nmh = -mhat;
#pragma unroll
    for (int r = 0; r < 16; ++r) { pA0[r] = EX(pA0[r]); pA1[r] = EX(pA1[r]); } }

#define GAPA(i, t, MF, a0, a1, W, PW) do { VRD(i, (t) - 1); SBAR(); MF; sacc += a0; sacc2 += a1; W; PIN(PW); PIN(sacc); PIN(sacc2); SBAR(); } while (0)
#define GAPB(i, t, GL, MF, X, j) do { if (GL) { KRD(i, (t) + 1); SBAR(); } MF; X[j] = EX(X[j]); X[j + 1] = EX(X[j + 1]); PIN(X); SBAR(); } while (0)
#define STEP(C0, C1, P0, P1, t, GL) do { SBAR(); \
    float sacc = 0.f, sacc2 = 0.f; \
    GAPA(0, t,  C0 = MFMA(kf[0], qr[0], zero16), P0[0], P0[1],   pw0[0] = PKW(P0, 0), pw0); \
    GAPA(1, t,  C1 = MFMA(kf[1], qr[0], zero16), P0[2], P0[3],   pw0[1] = PKW(P0, 2), pw0); \
    GAPA(2, t,  C0 = MFMA(kf[2], qr[1], C0),     P0[4], P0[5],   pw0[2] = PKW(P0, 4), pw0); \
    GAPA(3, t,  C1 = MFMA(kf[3], qr[1], C1),     P0[6], P0[7],   pw0[3] = PKW(P0, 6), pw0); \
    GAPA(4, t,  C0 = MFMA(kf[4], qr[2], C0),     P0[8], P0[9],   pw1[0] = PKW(P0, 8), pw1); \
    GAPA(5, t,  C1 = MFMA(kf[5], qr[2], C1),     P0[10], P0[11], pw1[1] = PKW(P0, 10), pw1); \
    GAPA(6, t,  C0 = MFMA(kf[6], qr[3], C0),     P0[12], P0[13], pw1[2] = PKW(P0, 12), pw1); \
    GAPA(7, t,  C1 = MFMA(kf[7], qr[3], C1),     P0[14], P0[15], pw1[3] = PKW(P0, 14), pw1); \
    GAPA(8, t,  C0 = MFMA(kf[8], qr[4], C0),     P1[0], P1[1],   pw2[0] = PKW(P1, 0), pw2); \
    GAPA(9, t,  C1 = MFMA(kf[9], qr[4], C1),     P1[2], P1[3],   pw2[1] = PKW(P1, 2), pw2); \
    GAPA(10, t, C0 = MFMA(kf[10], qr[5], C0),    P1[4], P1[5],   pw2[2] = PKW(P1, 4), pw2); \
    GAPA(11, t, C1 = MFMA(kf[11], qr[5], C1),    P1[6], P1[7],   pw2[3] = PKW(P1, 6), pw2); \
    GAPA(12, t, C0 = MFMA(kf[12], qr[6], C0),    P1[8], P1[9],   pw3[0] = PKW(P1, 8), pw3); \
    GAPA(13, t, C1 = MFMA(kf[13], qr[6], C1),    P1[10], P1[11], pw3[1] = PKW(P1, 10), pw3); \
    GAPA(14, t, C0 = MFMA(kf[14], qr[7], C0),    P1[12], P1[13], pw3[2] = PKW(P1, 12), pw3); \
    GAPA(15, t, C1 = MFMA(kf[15], qr[7], C1),    P1[14], P1[15], pw3[3] = PKW(P1, 14), pw3); \
    l_reg += sacc + sacc2; \
    float fsc = 1.f; bool resc = false; \
    { const float rm = __builtin_fmaf(rowmax(C0, C1), C2, -mhat); \
      if (__builtin_expect(__any(rm > THR), 0)) { const float dl = __builtin_fmaxf(rm, 0.f); mhat += dl; \
          fsc = __builtin_amdgcn_exp2f(-dl); l_reg *= fsc; resc = true; } } \
    const float nmh = -mhat; SBAR(); \
    GAPB(0, t, GL,  o0 = MFMA(VFR(0), PAF(0), o0), C0, 0); \
    GAPB(1, t, GL,  o1 = MFMA(VFR(1), PAF(0), o1), C0, 2); \
    GAPB(2, t, GL,  o2 = MFMA(VFR(2), PAF(0), o2), C0, 4); \
    GAPB(3, t, GL,  o3 = MFMA(VFR(3), PAF(0), o3), C0, 6); \
    GAPB(4, t, GL,  o0 = MFMA(VFR(4), PAF(1), o0), C0, 8); \
    GAPB(5, t, GL,  o1 = MFMA(VFR(5), PAF(1), o1), C0, 10); \
    GAPB(6, t, GL,  o2 = MFMA(VFR(6), PAF(1), o2), C0, 12); \
    GAPB(7, t, GL,  o3 = MFMA(VFR(7), PAF(1), o3), C0, 14); \
    GAPB(8, t, GL,  o0 = MFMA(VFR(8), PAF(2), o0), C1, 0); \
    GAPB(9, t, GL,  o1 = MFMA(VFR(9), PAF(2), o1), C1, 2); \
    GAPB(10, t, GL, o2 = MFMA(VFR(10), PAF(2), o2), C1, 4); \
    GAPB(11, t, GL, o3 = MFMA(VFR(11), PAF(2), o3), C1, 6); \
    GAPB(12, t, GL, o0 = MFMA(VFR(12), PAF(3), o0), C1, 8); \
    GAPB(13, t, GL, o1 = MFMA(VFR(13), PAF(3), o1), C1, 10); \
    GAPB(14, t, GL, o2 = MFMA(VFR(14), PAF(3), o2), C1, 12); \
    GAPB(15, t, GL, o3 = MFMA(VFR(15), PAF(3), o3), C1, 14); \
    if (__builtin_expect(resc, 0)) { o0 *= fsc; o1 *= fsc; o2 *= fsc; o3 *= fsc; } \
    } while (0)

  WAIT_BAR(20); DMA_K(5);
  STEP(pB0, pB1, pA0, pA1, 1, true);
  WAIT_BAR(16); DMA_K(6); DMA_V(4);
  STEP(pA0, pA1, pB0, pB1, 2, true);
  WAIT_BAR(12); DMA_K(7); DMA_V(5);
  STEP(pB0, pB1, pA0, pA1, 3, true);
  WAIT_BAR(16); DMA_V(6);
  STEP(pA0, pA1, pB0, pB1, 4, true);
  WAIT_BAR(12); DMA_V(7);
  STEP(pB0, pB1, pA0, pA1, 5, true);
  WAIT_BAR(8);
  STEP(pA0, pA1, pB0, pB1, 6, true);
  WAIT_BAR(4);
  STEP(pB0, pB1, pA0, pA1, 7, false);
  { float sacc = 0.f, sacc2 = 0.f;
#pragma unroll
    for (int r = 0; r < 16; ++r) { sacc += pB0[r]; sacc2 += pB1[r]; }
    l_reg += sacc + sacc2;
    pw0 = (u32x4){PKW(pB0, 0), PKW(pB0, 2), PKW(pB0, 4), PKW(pB0, 6)}; pw1 = (u32x4){PKW(pB0, 8), PKW(pB0, 10), PKW(pB0, 12), PKW(pB0, 14)};
    pw2 = (u32x4){PKW(pB1, 0), PKW(pB1, 2), PKW(pB1, 4), PKW(pB1, 6)}; pw3 = (u32x4){PKW(pB1, 8), PKW(pB1, 10), PKW(pB1, 12), PKW(pB1, 14)};
    PIN(pw0); PIN(pw1); PIN(pw2); PIN(pw3); PIN(l_reg);
  }
  WAIT_BAR(0);
#pragma unroll
  for (int i = 0; i < 16; ++i) VRD(i, 7);
  o0 = MFMA(VFR(0), PAF(0), o0); o1 = MFMA(VFR(1), PAF(0), o1); o2 = MFMA(VFR(2), PAF(0), o2); o3 = MFMA(VFR(3), PAF(0), o3);
  o0 = MFMA(VFR(4), PAF(1), o0); o1 = MFMA(VFR(5), PAF(1), o1); o2 = MFMA(VFR(6), PAF(1), o2); o3 = MFMA(VFR(7), PAF(1), o3);
  o0 = MFMA(VFR(8), PAF(2), o0); o1 = MFMA(VFR(9), PAF(2), o1); o2 = MFMA(VFR(10), PAF(2), o2); o3 = MFMA(VFR(11), PAF(2), o3);
  o0 = MFMA(VFR(12), PAF(3), o0); o1 = MFMA(VFR(13), PAF(3), o1); o2 = MFMA(VFR(14), PAF(3), o2); o3 = MFMA(VFR(15), PAF(3), o3);

  { auto rr = __builtin_amdgcn_permlane32_swap(__float_as_uint(l_reg), __float_as_uint(l_reg), false, false);
    l_reg = __uint_as_float(rr[0]) + __uint_as_float(rr[1]); }
  const float il = __builtin_amdgcn_rcpf(l_reg);
  char* orow = (char*)O + ((size_t)(pair * 2 + half) * 128 + wid * 32 + r32) * 256 + hi * 16;
#define OST(oo, db) do { _Pragma("unroll") for (int g = 0; g < 4; g += 2) { \
      unsigned a0 = cvtpk(oo[4 * g] * il, oo[4 * g + 1] * il), a1 = cvtpk(oo[4 * g + 2] * il, oo[4 * g + 3] * il); \
      unsigned b0 = cvtpk(oo[4 * g + 4] * il, oo[4 * g + 5] * il), b1 = cvtpk(oo[4 * g + 6] * il, oo[4 * g + 7] * il); \
      auto r0 = __builtin_amdgcn_permlane32_swap(a0, b0, false, false); auto r1 = __builtin_amdgcn_permlane32_swap(a1, b1, false, false); \
      *(u32x4*)(orow + 64 * (db) + 16 * g) = (u32x4){r0[0], r1[0], r0[1], r1[1]}; } } while (0)
  OST(o0, 0); OST(o1, 1); OST(o2, 2); OST(o3, 3);
}

extern "C" void kernel_launch(void* const* d_in, const int* in_sizes, int n_in, void* d_out, int out_size, void* d_ws, size_t ws_size, hipStream_t stream) {
  gqa_fwd<<<256, 256, 0, stream>>>((const unsigned short*)d_in[0], (const unsigned short*)d_in[1], (const unsigned short*)d_in[2], (unsigned short*)d_out);
}
```

```cpp
#include <hip/hip_runtime.h>
#include <hip/hip_bf16.h>

typedef __bf16 bf16x8 __attribute__((ext_vector_type(8)));
typedef float f32x16 __attribute__((ext_vector_type(16)));
typedef float f32x2 __attribute__((ext_vector_type(2)));
typedef __bf16 bf16x2 __attribute__((ext_vector_type(2)));
typedef unsigned u32x4 __attribute__((ext_vector_type(4)));
typedef unsigned u32x2 __attribute__((ext_vector_type(2)));
typedef __attribute__((address_space(3))) char* lds_ptr;

__device__ __forceinline__ unsigned cvtpk(float lo, float hi) { f32x2 v = {lo, hi}; bf16x2 b = __builtin_convertvector(v, bf16x2); return __builtin_bit_cast(unsigned, b); }

__device__ __forceinline__ void safe_rows(const unsigned short* __restrict__ q, const unsigned short* __restrict__ k,
                                          const unsigned short* __restrict__ v, unsigned short* __restrict__ o, unsigned lane) {
  const unsigned r = lane & 31u, h = lane >> 5;
  const float c = 0.12751743082459868f;
  bf16x8 qf[8];
#pragma unroll
  for (int d0 = 0; d0 < 8; ++d0) qf[d0] = *(const bf16x8*)(q + r * 128 + d0 * 16 + h * 8);
  f32x16 acc[4] = {};
  float m = -__builtin_inff(), l = 0.f;
#pragma unroll 1
  for (int t = 0; t < 16; ++t) {
    f32x16 s = {};
#pragma unroll
    for (int d0 = 0; d0 < 8; ++d0) s = __builtin_amdgcn_mfma_f32_32x32x16_bf16(*(const bf16x8*)(k + (t * 32 + r) * 128 + d0 * 16 + h * 8), qf[d0], s, 0, 0, 0);
    float mx = s[0];
#pragma unroll
    for (int i = 1; i < 16; ++i) mx = fmaxf(mx, s[i]);
    mx = fmaxf(mx, __shfl_xor(mx, 32));
    const float mn = fmaxf(m, mx * c);
    const float ms = (mn == -__builtin_inff()) ? 0.f : mn;
    const float al = __builtin_amdgcn_exp2f(m - ms);
    m = mn;
    float ps = 0.f;
#pragma unroll
    for (int i = 0; i < 16; ++i) { s[i] = __builtin_amdgcn_exp2f(s[i] * c - ms); ps += s[i]; }
    l = l * al + ps;
#pragma unroll
    for (int db = 0; db < 4; ++db) acc[db] *= al;
#pragma unroll
    for (int ks = 0; ks < 2; ++ks) {
      const u32x4 pw = {cvtpk(s[8 * ks], s[8 * ks + 1]), cvtpk(s[8 * ks + 2], s[8 * ks + 3]), cvtpk(s[8 * ks + 4], s[8 * ks + 5]), cvtpk(s[8 * ks + 6], s[8 * ks + 7])};
#pragma unroll
      for (int db = 0; db < 4; ++db) {
        const unsigned short* vp = v + (t * 32 + 16 * ks + 4 * h) * 128 + db * 32 + r;
        const u32x4 vw = {vp[0] | ((unsigned)vp[128] << 16), vp[256] | ((unsigned)vp[384] << 16),
                          vp[1024] | ((unsigned)vp[1152] << 16), vp[1280] | ((unsigned)vp[1408] << 16)};
        acc[db] = __builtin_amdgcn_mfma_f32_32x32x16_bf16(__builtin_bit_cast(bf16x8, vw), __builtin_bit_cast(bf16x8, pw), acc[db], 0, 0, 0);
      }
    }
  }
  l += __shfl_xor(l, 32);
  const float il = 1.0f / l;
#pragma unroll
  for (int db = 0; db < 4; ++db)
#pragma unroll
    for (int g = 0; g < 4; ++g)
      *(u32x2*)(o + r * 128 + db * 32 + g * 8 + h * 4) = (u32x2){cvtpk(acc[db][4 * g] * il, acc[db][4 * g + 1] * il), cvtpk(acc[db][4 * g + 2] * il, acc[db][4 * g + 3] * il)};
}

struct KArgs { const unsigned short* Q; const unsigned short* K; const unsigned short* V; unsigned short* O; };
typedef __attribute__((address_space(4))) const KArgs* kargs_ptr;

__global__ __launch_bounds__(256) __attribute__((aligned(4096)))
void gqa_fwd(const unsigned short* Q, const unsigned short* K, const unsigned short* V, unsigned short* O) {
  __shared__ __attribute__((aligned(16))) char lds[163840];
  unsigned bid = blockIdx.x, tid = threadIdx.x;
  kargs_ptr ka = (kargs_ptr)__builtin_amdgcn_kernarg_segment_ptr();
  asm volatile(
    ".Lq_entry_%=:\n"
    "s_load_dwordx8 s[8:15], %0, 0\n"
    "s_getpc_b64 s[52:53]\n"
    "v_lshlrev_b32 v8, 6, %2\n"
    "v_readfirstlane_b32 s46, %2\n"
    "v_min_u32 v8, .Lq_fb_%=-.Lq_entry_%=+1280, v8\n"
    "global_load_dword v144, v8, s[52:53]\n"
    "v_and_b32 v4, 63, %2\n"
    "s_lshr_b32 s46, s46, 6\n"
    "s_and_b32 s42, %1, 7\n"
    "s_lshl_b32 s42, s42, 4\n"
    "s_lshr_b32 s43, %1, 4\n"
    "s_add_u32 s42, s42, s43\n"
    "s_bfe_u32 s43, %1, 0x10003\n"
    "s_lshl_b32 s47, s42, 1\n"
    "s_add_u32 s47, s47, s43\n"
    "s_lshl_b32 s47, s47, 2\n"
    "s_add_u32 s47, s47, s46\n"
    "s_lshl_b32 s47, s47, 13\n"
    "s_lshl_b32 s42, s42, 17\n"
    "s_and_b32 s34, s46, 1\n"
    "s_lshl_b32 s45, s34, 16\n"
    "s_lshl_b32 s34, s34, 10\n"
    "s_lshr_b32 s35, s46, 1\n"
    "s_lshl_b32 s43, s35, 16\n"
    "s_add_u32 s34, s34, s43\n"
    "s_xor_b32 s35, s35, 1\n"
    "s_lshl_b32 s35, s35, 11\n"
    "v_bfe_u32 v8, v4, 2, 2\n"
    "v_bfe_u32 v6, v4, 2, 1\n"
    "v_lshlrev_b32 v15, 15, v6\n"
    "v_bfe_u32 v6, v4, 3, 1\n"
    "v_lshl_add_u32 v15, v6, 8, v15\n"
    "v_lshrrev_b32 v6, 4, v4\n"
    "v_lshl_add_u32 v15, v6, 6, v15\n"
    "v_and_b32 v9, 3, v4\n"
    "v_lshl_add_u32 v15, v9, 4, v15\n"
    "v_add_u32 v12, 3120, v15\n"
    "v_add_u32 v13, 2592, v15\n"
    "v_add_u32 v14, 2064, v15\n"
    "v_add_u32 v15, 1536, v15\n"
    "v_lshrrev_b32 v7, 3, v4\n"
    "v_lshlrev_b32 v16, 8, v7\n"
    "v_and_b32 v7, 7, v4\n"
    "v_lshl_add_u32 v16, v7, 4, v16\n"
    "v_lshlrev_b32 v6, 5, v6\n"
    "v_xor_b32 v16, v16, v6\n"
    "v_add_u32 v17, 1008, v16\n"
    "v_add_u32 v18, 2016, v16\n"
    "v_add_u32 v19, 3024, v16\n"
    "s_mov_b32 s23, 0x20000\n"
    "s_mov_b32 s22, 134192\n"
    "s_mov_b32 s19, s23\n"
    "s_movk_i32 s18, 0x2000\n"
    "s_mul_i32 s44, s46, 2080\n"
    "s_mul_i32 s48, s46, 4160\n"
    "s_sub_u32 s42, s42, 3120\n"
    "s_subb_u32 s43, 0, 0\n"
    "s_add_u32 m0, s44, 33280\n"
    "s_movk_i32 s49, 0x80\n"
    "s_waitcnt lgkmcnt(0)\n"
    "s_add_u32 s29, s42, 3120\n"
    "s_load_dword s52, s[8:9], s47\n"
    "s_load_dword s53, s[10:11], s29\n"
    "s_load_dword s52, s[12:13], s29\n"
    "s_add_u32 s20, s10, s42\n"
    "s_addc_u32 s21, s11, s43\n"
    "buffer_load_dwordx4 v12, s[20:23], s34 offen offset:0 nt lds\n"
    "buffer_load_dwordx4 v13, s[20:23], s34 offen offset:1040 nt lds\n"
    "s_add_u32 s16, s8, s47\n"
    "s_addc_u32 s17, s9, 0\n"
    "s_lshl_b32 m0, s48, 1\n"
    "s_add_u32 s24, s12, s42\n"
    "buffer_load_dwordx4 v16, s[16:19], 0 offen offset:0 nt lds\n"
    "buffer_load_dwordx4 v17, s[16:19], 0 offen offset:1040 nt lds\n"
    "buffer_load_dwordx4 v18, s[16:19], 0 offen offset:2080 nt lds\n"
    "buffer_load_dwordx4 v19, s[16:19], 0 offen offset:3120 nt lds\n"
    "s_addc_u32 s25, s13, s43\n"
    "s_add_u32 m0, m0, 4160\n"
    "s_mov_b64 s[26:27], s[22:23]\n"
    "s_barrier\n"
    "buffer_load_dwordx4 v16, s[16:19], s49 offen offset:0 nt lds\n"
    "buffer_load_dwordx4 v17, s[16:19], s49 offen offset:1040 nt lds\n"
    "buffer_load_dwordx4 v18, s[16:19], s49 offen offset:2080 nt lds\n"
    "buffer_load_dwordx4 v19, s[16:19], s49 offen offset:3120 nt lds\n"
    "s_cmp_lt_u32 s46, 2\n"
    "s_cselect_b64 s[28:29], s[20:21], s[24:25]\n"
    "s_mov_b64 s[30:31], s[22:23]\n"
    "s_add_u32 s28, s28, s45\n"
    "s_addc_u32 s29, s29, 0\n"
    "s_sub_u32 s30, s30, s45\n"
    "s_add_u32 s36, s14, s47\n"
    "s_addc_u32 s37, s15, 0\n"
    "s_mov_b64 s[38:39], s[18:19]\n"
    "v_bfe_u32 v6, v4, 2, 3\n"
    "v_mul_u32_u24 v240, 1040, v6\n"
    "v_lshl_add_u32 v240, v9, 6, v240\n"
    "v_lshrrev_b32 v6, 5, v4\n"
    "v_lshl_add_u32 v240, v6, 4, v240\n"
    "v_mul_u32_u24 v243, 1040, v6\n"
    "v_lshl_add_u32 v243, v9, 3, v243\n"
    "v_lshl_add_u32 v243, v8, 6, v243\n"
    "v_bfe_u32 v8, v4, 4, 1\n"
    "v_lshl_add_u32 v243, v8, 5, v243\n"
    "v_add_u32 v241, 58240, v240\n"
    "v_add_u32 v244, 58240, v243\n"
    "v_add_u32 v242, 116480, v240\n"
    "v_add_u32 v245, 116480, v243\n"
    "v_and_b32 v246, 28, v4\n"
    "v_lshlrev_b32 v246, 8, v246\n"
    "v_lshl_add_u32 v246, v9, 4, v246\n"
    "v_lshl_add_u32 v246, v6, 9, v246\n"
    "v_bfe_u32 v8, v4, 3, 2\n"
    "v_mul_u32_u24 v8, 1040, v8\n"
    "v_lshl_add_u32 v8, v7, 7, v8\n"
    "v_lshl_add_u32 v8, s48, 1, v8\n"
    "v_and_or_b32 v9, v4, 6, v6\n"
    "v_lshl_add_u32 v20, v9, 4, v8\n"
    "v_xor_b32 v7, 2, v9\n"
    "v_lshl_add_u32 v21, v7, 4, v8\n"
    "v_xor_b32 v7, 4, v9\n"
    "v_lshl_add_u32 v22, v7, 4, v8\n"
    "v_xor_b32 v7, 6, v9\n"
    "v_lshl_add_u32 v23, v7, 4, v8\n"
    "v_lshrrev_b32 v8, 2, v4\n"
    "v_xor_b32 v8, v8, v4\n"
    "v_bfe_i32 v8, v8, 2, 1\n"
    "v_not_b32 v8, v8\n"
    "v_and_b32 v8, 0x3f803f80, v8\n"
    "v_accvgpr_write_b32 a64, v8\n"
    "v_accvgpr_write_b32 a65, v8\n"
    "v_accvgpr_write_b32 a66, v8\n"
    "v_accvgpr_write_b32 a67, v8\n"
    "v_mov_b64 v[10:11], 0\n"
    "s_mov_b32 s40, 0x3e0293ee\n"
    "s_mov_b32 s41, s40\n"
    "s_mov_b32 s50, 0xffff0000\n"
    "s_mov_b32 s51, 0x4e800000\n"
    "s_barrier\n"
    "s_add_u32 m0, s44, 41600\n"
    "s_add_u32 s49, s34, 2048\n"
    "buffer_load_dwordx4 v12, s[20:23], s49 offen offset:0 nt lds\n"
    "buffer_load_dwordx4 v13, s[20:23], s49 offen offset:1040 nt lds\n"
    "s_add_u32 m0, s44, 49920\n"
    "s_mov_b32 s49, s34\n"
    "buffer_load_dwordx4 v12, s[24:27], s49 offen offset:0 nt lds\n"
    "buffer_load_dwordx4 v13, s[24:27], s49 offen offset:1040 nt lds\n"
    "s_mov_b64 exec, 0\n"
    "s_cmp_eq_u32 s46, 1\n"
    "s_cbranch_scc1 .Lq_tp0_%=\n"
    "s_cmp_eq_u32 s46, 2\n"
    "s_cbranch_scc1 .Lq_tp1_%=\n"
    "s_cmp_eq_u32 s46, 3\n"
    "s_cbranch_scc1 .Lq_tret_%=\n"
    ".Lq_tret_%=:\n"
    "s_mov_b64 exec, -1\n"
    "s_waitcnt vmcnt(8)\n"
    "ds_read_b128 v[80:83], v20 offset:0\n"
    "ds_read_b128 v[84:87], v21 offset:0\n"
    "ds_read_b128 v[88:91], v22 offset:0\n"
    "ds_read_b128 v[92:95], v23 offset:0\n"
    "s_barrier\n"
    "ds_read_b128 a[32:35], v240 offset:33280\n"
    "ds_read_b128 a[36:39], v240 offset:33312\n"
    "ds_read_b128 a[40:43], v240 offset:33536\n"
    "ds_read_b128 a[44:47], v240 offset:33568\n"
    "ds_read_b128 a[48:51], v240 offset:33792\n"
    "ds_read_b128 a[52:55], v240 offset:33824\n"
    "ds_read_b128 a[56:59], v240 offset:34048\n"
    "ds_read_b128 a[60:63], v240 offset:34080\n"
    "s_waitcnt lgkmcnt(11)\n"
    "v_lshlrev_b32 v176, 16, v80\n"
    "v_and_b32 v177, s50, v80\n"
    "v_lshlrev_b32 v178, 16, v81\n"
    "v_and_b32 v179, s50, v81\n"
    "v_lshlrev_b32 v180, 16, v82\n"
    "v_and_b32 v181, s50, v82\n"
    "v_lshlrev_b32 v182, 16, v83\n"
    "v_and_b32 v183, s50, v83\n"
    "v_pk_mul_f32 v[176:177], v[176:177], s[40:41]\n"
    "v_pk_mul_f32 v[178:179], v[178:179], s[40:41]\n"
    "v_pk_mul_f32 v[180:181], v[180:181], s[40:41]\n"
    "v_pk_mul_f32 v[182:183], v[182:183], s[40:41]\n"
    "v_cvt_pk_bf16_f32 v80, v176, v177\n"
    "v_cvt_pk_bf16_f32 v81, v178, v179\n"
    "v_cvt_pk_bf16_f32 v82, v180, v181\n"
    "v_cvt_pk_bf16_f32 v83, v182, v183\n"
    "s_waitcnt lgkmcnt(7)\n"
    "v_lshlrev_b32 v176, 16, v84\n"
    "v_and_b32 v177, s50, v84\n"
    "v_mfma_f32_32x32x16_bf16 v[112:127], a[32:35], v[80:83], 0\n"
    "s_add_u32 m0, s48, 58240\n"
    "s_add_u32 s35, s35, 2048\n"
    "buffer_load_dwordx4 v12, s[28:31], s35 offen offset:0 nt lds\n"
    "buffer_load_dwordx4 v13, s[28:31], s35 offen offset:1040 nt lds\n"
    "v_lshlrev_b32 v178, 16, v85\n"
    "v_and_b32 v179, s50, v85\n"
    "v_lshlrev_b32 v180, 16, v86\n"
    "v_and_b32 v181, s50, v86\n"
    "v_lshlrev_b32 v182, 16, v87\n"
    "v_and_b32 v183, s50, v87\n"
    "v_pk_mul_f32 v[176:177], v[176:177], s[40:41]\n"
    "v_pk_mul_f32 v[178:179], v[178:179], s[40:41]\n"
    "v_pk_mul_f32 v[180:181], v[180:181], s[40:41]\n"
    "v_pk_mul_f32 v[182:183], v[182:183], s[40:41]\n"
    "v_cvt_pk_bf16_f32 v84, v176, v177\n"
    "v_cvt_pk_bf16_f32 v85, v178, v179\n"
    "v_cvt_pk_bf16_f32 v86, v180, v181\n"
    "v_cvt_pk_bf16_f32 v87, v182, v183\n"
    "s_waitcnt lgkmcnt(0)\n"
    "v_lshlrev_b32 v176, 16, v88\n"
    "v_and_b32 v177, s50, v88\n"
    "v_mfma_f32_32x32x16_bf16 v[112:127], a[36:39], v[84:87], v[112:127]\n"
    "buffer_load_dwordx4 v14, s[28:31], s35 offen offset:2080 nt lds\n"
    "buffer_load_dwordx4 v15, s[28:31], s35 offen offset:3120 nt lds\n"
    "v_lshlrev_b32 v178, 16, v89\n"
    "v_and_b32 v179, s50, v89\n"
    "v_lshlrev_b32 v180, 16, v90\n"
    "v_and_b32 v181, s50, v90\n"
    "v_lshlrev_b32 v182, 16, v91\n"
    "v_and_b32 v183, s50, v91\n"
    "v_pk_mul_f32 v[176:177], v[176:177], s[40:41]\n"
    "v_pk_mul_f32 v[178:179], v[178:179], s[40:41]\n"
    "v_pk_mul_f32 v[180:181], v[180:181], s[40:41]\n"
    "v_pk_mul_f32 v[182:183], v[182:183], s[40:41]\n"
    "v_cvt_pk_bf16_f32 v88, v176, v177\n"
    "v_cvt_pk_bf16_f32 v89, v178, v179\n"
    "v_cvt_pk_bf16_f32 v90, v180, v181\n"
    "v_cvt_pk_bf16_f32 v91, v182, v183\n"
    "v_lshlrev_b32 v176, 16, v92\n"
    "v_and_b32 v177, s50, v92\n"
    "v_mfma_f32_32x32x16_bf16 v[112:127], a[40:43], v[88:91], v[112:127]\n"
    "s_waitcnt vmcnt(8)\n"
    "ds_read_b128 v[96:99], v20 offset:4160\n"
    "ds_read_b128 v[100:103], v21 offset:4160\n"
    "ds_read_b128 v[104:107], v22 offset:4160\n"
    "ds_read_b128 v[108:111], v23 offset:4160\n"
    "v_lshlrev_b32 v178, 16, v93\n"
    "v_and_b32 v179, s50, v93\n"
    "v_lshlrev_b32 v180, 16, v94\n"
    "v_and_b32 v181, s50, v94\n"
    "v_lshlrev_b32 v182, 16, v95\n"
    "v_and_b32 v183, s50, v95\n"
    "v_pk_mul_f32 v[176:177], v[176:177], s[40:41]\n"
    "v_pk_mul_f32 v[178:179], v[178:179], s[40:41]\n"
    "v_pk_mul_f32 v[180:181], v[180:181], s[40:41]\n"
    "v_pk_mul_f32 v[182:183], v[182:183], s[40:41]\n"
    "v_cvt_pk_bf16_f32 v92, v176, v177\n"
    "v_cvt_pk_bf16_f32 v93, v178, v179\n"
    "v_cvt_pk_bf16_f32 v94, v180, v181\n"
    "v_cvt_pk_bf16_f32 v95, v182, v183\n"
    "s_waitcnt lgkmcnt(3)\n"
    "v_lshlrev_b32 v176, 16, v96\n"
    "v_and_b32 v177, s50, v96\n"
    "v_mfma_f32_32x32x16_bf16 v[112:127], a[44:47], v[92:95], v[112:127]\n"
    "s_add_u32 m0, s48, 74880\n"
    "s_add_u32 s35, s35, 2048\n"
    "buffer_load_dwordx4 v12, s[28:31], s35 offen offset:0 nt lds\n"
    "v_lshlrev_b32 v178, 16, v97\n"
    "v_and_b32 v179, s50, v97\n"
    "v_lshlrev_b32 v180, 16, v98\n"
    "v_and_b32 v181, s50, v98\n"
    "v_lshlrev_b32 v182, 16, v99\n"
    "v_and_b32 v183, s50, v99\n"
    "v_pk_mul_f32 v[176:177], v[176:177], s[40:41]\n"
    "v_pk_mul_f32 v[178:179], v[178:179], s[40:41]\n"
    "v_pk_mul_f32 v[180:181], v[180:181], s[40:41]\n"
    "v_pk_mul_f32 v[182:183], v[182:183], s[40:41]\n"
    "v_cvt_pk_bf16_f32 v96, v176, v177\n"
    "v_cvt_pk_bf16_f32 v97, v178, v179\n"
    "v_cvt_pk_bf16_f32 v98, v180, v181\n"
    "v_cvt_pk_bf16_f32 v99, v182, v183\n"
    "s_waitcnt lgkmcnt(0)\n"
    "v_lshlrev_b32 v176, 16, v100\n"
    "v_and_b32 v177, s50, v100\n"
    "v_mfma_f32_32x32x16_bf16 v[112:127], a[48:51], v[96:99], v[112:127]\n"
    "buffer_load_dwordx4 v13, s[28:31], s35 offen offset:1040 nt lds\n"
    "v_lshlrev_b32 v178, 16, v101\n"
    "v_and_b32 v179, s50, v101\n"
    "v_lshlrev_b32 v180, 16, v102\n"
    "v_and_b32 v181, s50, v102\n"
    "v_lshlrev_b32 v182, 16, v103\n"
    "v_and_b32 v183, s50, v103\n"
    "v_pk_mul_f32 v[176:177], v[176:177], s[40:41]\n"
    "v_pk_mul_f32 v[178:179], v[178:179], s[40:41]\n"
    "v_pk_mul_f32 v[180:181], v[180:181], s[40:41]\n"
    "v_pk_mul_f32 v[182:183], v[182:183], s[40:41]\n"
    "v_cvt_pk_bf16_f32 v100, v176, v177\n"
    "v_cvt_pk_bf16_f32 v101, v178, v179\n"
    "v_cvt_pk_bf16_f32 v102, v180, v181\n"
    "v_cvt_pk_bf16_f32 v103, v182, v183\n"
    "v_lshlrev_b32 v176, 16, v104\n"
    "v_and_b32 v177, s50, v104\n"
    "v_mfma_f32_32x32x16_bf16 v[112:127], a[52:55], v[100:103], v[112:127]\n"
    "buffer_load_dwordx4 v14, s[28:31], s35 offen offset:2080 nt lds\n"
    "v_lshlrev_b32 v178, 16, v105\n"
    "v_and_b32 v179, s50, v105\n"
    "v_lshlrev_b32 v180, 16, v106\n"
    "v_and_b32 v181, s50, v106\n"
    "v_lshlrev_b32 v182, 16, v107\n"
    "v_and_b32 v183, s50, v107\n"
    "v_pk_mul_f32 v[176:177], v[176:177], s[40:41]\n"
    "v_pk_mul_f32 v[178:179], v[178:179], s[40:41]\n"
    "v_pk_mul_f32 v[180:181], v[180:181], s[40:41]\n"
    "v_pk_mul_f32 v[182:183], v[182:183], s[40:41]\n"
    "v_cvt_pk_bf16_f32 v104, v176, v177\n"
    "v_cvt_pk_bf16_f32 v105, v178, v179\n"
    "v_cvt_pk_bf16_f32 v106, v180, v181\n"
    "v_cvt_pk_bf16_f32 v107, v182, v183\n"
    "v_lshlrev_b32 v176, 16, v108\n"
    "v_and_b32 v177, s50, v108\n"
    "v_mfma_f32_32x32x16_bf16 v[112:127], a[56:59], v[104:107], v[112:127]\n"
    "buffer_load_dwordx4 v15, s[28:31], s35 offen offset:3120 nt lds\n"
    "v_lshlrev_b32 v178, 16, v109\n"
    "v_and_b32 v179, s50, v109\n"
    "v_lshlrev_b32 v180, 16, v110\n"
    "v_and_b32 v181, s50, v110\n"
    "v_lshlrev_b32 v182, 16, v111\n"
    "v_and_b32 v183, s50, v111\n"
    "v_pk_mul_f32 v[176:177], v[176:177], s[40:41]\n"
    "v_pk_mul_f32 v[178:179], v[178:179], s[40:41]\n"
    "v_pk_mul_f32 v[180:181], v[180:181], s[40:41]\n"
    "v_pk_mul_f32 v[182:183], v[182:183], s[40:41]\n"
    "v_cvt_pk_bf16_f32 v108, v176, v177\n"
    "v_cvt_pk_bf16_f32 v109, v178, v179\n"
    "v_cvt_pk_bf16_f32 v110, v180, v181\n"
    "v_cvt_pk_bf16_f32 v111, v182, v183\n"
    "s_nop 1\n"
    "v_mfma_f32_32x32x16_bf16 v[112:127], a[60:63], v[108:111], v[112:127]\n"
    "s_waitcnt vmcnt(10)\n"
    "s_barrier\n"
    "ds_read_b128 a[0:3], v240 offset:41600\n"
    "ds_read_b128 a[4:7], v240 offset:41632\n"
    "ds_read_b128 a[8:11], v240 offset:41856\n"
    "ds_read_b128 a[12:15], v240 offset:41888\n"
    "ds_read_b128 a[16:19], v240 offset:42112\n"
    "ds_read_b128 a[20:23], v240 offset:42144\n"
    "ds_read_b128 a[24:27], v240 offset:42368\n"
    "ds_read_b128 a[28:31], v240 offset:42400\n"
    "s_add_u32 m0, s48, 91520\n"
    "s_add_u32 s35, s35, 2048\n"
    "buffer_load_dwordx4 v12, s[28:31], s35 offen offset:0 nt lds\n"
    "buffer_load_dwordx4 v13, s[28:31], s35 offen offset:1040 nt lds\n"
    "buffer_load_dwordx4 v14, s[28:31], s35 offen offset:2080 nt lds\n"
    "buffer_load_dwordx4 v15, s[28:31], s35 offen offset:3120 nt lds\n"
    "v_max3_f32 v6, v112, v113, v114\n"
    "v_max3_f32 v7, v115, v116, v117\n"
    "v_max3_f32 v6, v6, v118, v119\n"
    "v_max3_f32 v7, v7, v120, v121\n"
    "v_max3_f32 v6, v6, v122, v123\n"
    "v_max3_f32 v7, v7, v124, v125\n"
    "v_max3_f32 v6, v6, v126, v127\n"
    "v_max_f32 v6, v6, v7\n"
    "v_mov_b32 v7, v6\n"
    "s_nop 1\n"
    "v_permlane32_swap_b32 v6, v7\n"
    "v_min_f32_e64 v144, -v6, -v7\n"
    "v_mov_b32 v145, v144\n"
    "v_mov_b64 v[146:147], v[144:145]\n"
    "v_mov_b64 v[148:149], v[144:145]\n"
    "v_mov_b64 v[150:151], v[144:145]\n"
    "v_mov_b64 v[152:153], v[144:145]\n"
    "v_mov_b64 v[154:155], v[144:145]\n"
    "v_mov_b64 v[156:157], v[144:145]\n"
    "v_mov_b64 v[158:159], v[144:145]\n"
    "v_pk_add_f32 v[112:113], v[112:113], v[144:145]\n"
    "v_pk_add_f32 v[114:115], v[114:115], v[144:145]\n"
    "v_pk_add_f32 v[116:117], v[116:117], v[144:145]\n"
    "v_pk_add_f32 v[118:119], v[118:119], v[144:145]\n"
    "v_pk_add_f32 v[120:121], v[120:121], v[144:145]\n"
    "v_pk_add_f32 v[122:123], v[122:123], v[144:145]\n"
    "v_pk_add_f32 v[124:125], v[124:125], v[144:145]\n"
    "v_pk_add_f32 v[126:127], v[126:127], v[144:145]\n"
    "s_waitcnt lgkmcnt(0)\n"
    "v_mfma_f32_32x32x16_bf16 v[128:143], a[0:3], v[80:83], v[144:159]\n"
    "s_add_u32 m0, s48, 108160\n"
    "s_add_u32 s35, s35, 2048\n"
    "buffer_load_dwordx4 v12, s[28:31], s35 offen offset:0 nt lds\n"
    "v_exp_f32 v112, v112\n"
    "v_exp_f32 v113, v113\n"
    "v_mfma_f32_32x32x16_bf16 v[128:143], a[4:7], v[84:87], v[128:143]\n"
    "s_waitcnt vmcnt(13)\n"
    "buffer_load_dwordx4 v13, s[28:31], s35 offen offset:1040 nt lds\n"
    "v_cvt_pk_bf16_f32 v160, v112, v113\n"
    "v_exp_f32 v114, v114\n"
    "v_exp_f32 v115, v115\n"
    "v_mfma_f32_32x32x16_bf16 v[128:143], a[8:11], v[88:91], v[128:143]\n"
    "s_barrier\n"
    "v_cvt_pk_bf16_f32 v161, v114, v115\n"
    "v_exp_f32 v116, v116\n"
    "v_mfma_f32_32x32x16_bf16 v[128:143], a[12:15], v[92:95], v[128:143]\n"
    "ds_read_b64_tr_b16 a[32:33], v243 offset:49920\n"
    "ds_read_b64_tr_b16 a[34:35], v243 offset:52000\n"
    "ds_read_b64_tr_b16 a[40:41], v243 offset:50176\n"
    "v_exp_f32 v117, v117\n"
    "v_exp_f32 v118, v118\n"
    "v_cvt_pk_bf16_f32 v162, v116, v117\n"
    "v_mfma_f32_32x32x16_bf16 v[128:143], a[16:19], v[96:99], v[128:143]\n"
    "ds_read_b64_tr_b16 a[42:43], v243 offset:52256\n"
    "ds_read_b64_tr_b16 a[44:45], v243 offset:50432\n"
    "ds_read_b64_tr_b16 a[46:47], v243 offset:52512\n"
    "buffer_load_dwordx4 v14, s[28:31], s35 offen offset:2080 nt lds\n"
    "v_exp_f32 v119, v119\n"
    "v_mfma_f32_32x32x16_bf16 v[128:143], a[20:23], v[100:103], v[128:143]\n"
    "ds_read_b64_tr_b16 a[48:49], v243 offset:50688\n"
    "ds_read_b64_tr_b16 a[50:51], v243 offset:52768\n"
    "ds_read_b64_tr_b16 a[52:53], v243 offset:54080\n"
    "v_cvt_pk_bf16_f32 v163, v118, v119\n"
    "v_exp_f32 v120, v120\n"
    "v_exp_f32 v121, v121\n"
    "v_mfma_f32_32x32x16_bf16 v[128:143], a[24:27], v[104:107], v[128:143]\n"
    "ds_read_b64_tr_b16 a[54:55], v243 offset:56160\n"
    "ds_read_b64_tr_b16 a[60:61], v243 offset:54336\n"
    "ds_read_b64_tr_b16 a[62:63], v243 offset:56416\n"
    "buffer_load_dwordx4 v15, s[28:31], s35 offen offset:3120 nt lds\n"
    "v_cvt_pk_bf16_f32 v164, v120, v121\n"
    "v_exp_f32 v122, v122\n"
    "v_mfma_f32_32x32x16_bf16 v[128:143], a[28:31], v[108:111], v[128:143]\n"
    "s_waitcnt vmcnt(12) lgkmcnt(6)\n"
    "ds_read_b64_tr_b16 a[0:1], v243 offset:54592\n"
    "ds_read_b64_tr_b16 a[2:3], v243 offset:56672\n"
    "v_exp_f32 v123, v123\n"
    "v_exp_f32 v124, v124\n"
    "v_cvt_pk_bf16_f32 v165, v122, v123\n"
    "v_mfma_f32_32x32x16_bf16 v[16:31], a[32:35], v[160:163], 0\n"
    "s_barrier\n"
    "ds_read_b64_tr_b16 a[4:5], v243 offset:54848\n"
    "ds_read_b64_tr_b16 a[6:7], v243 offset:56928\n"
    "v_mfma_f32_16x16x32_bf16 v[250:253], a[64:67], v[160:163], 0\n"
    "s_add_u32 m0, s48, 124800\n"
    "ds_read_b128 a[8:11], v241 offset:0\n"
    "v_mfma_f32_32x32x16_bf16 v[32:47], a[40:43], v[160:163], 0\n"
    "s_add_u32 s35, s35, 2048\n"
    "ds_read_b128 a[12:15], v241 offset:32\n"
    "v_exp_f32 v125, v125\n"
    "v_exp_f32 v126, v126\n"
    "v_cvt_pk_bf16_f32 v166, v124, v125\n"
    "v_mfma_f32_32x32x16_bf16 v[48:63], a[44:47], v[160:163], 0\n"
    "s_waitcnt lgkmcnt(2)\n"
    "ds_read_b128 a[16:19], v241 offset:256\n"
    "buffer_load_dwordx4 v12, s[28:31], s35 offen offset:0 nt lds\n"
    "v_exp_f32 v127, v127\n"
    "v_mfma_f32_32x32x16_bf16 v[64:79], a[48:51], v[160:163], 0\n"
    "ds_read_b128 a[20:23], v241 offset:288\n"
    "v_cvt_pk_bf16_f32 v167, v126, v127\n"
    "v_exp_f32 v128, v128\n"
    "v_exp_f32 v129, v129\n"
    "v_mfma_f32_32x32x16_bf16 v[16:31], a[52:55], v[164:167], v[16:31]\n"
    "ds_read_b128 a[24:27], v241 offset:512\n"
    "v_cvt_pk_bf16_f32 v168, v128, v129\n"
    "v_exp_f32 v130, v130\n"
    "v_exp_f32 v131, v131\n"
    "v_mfma_f32_16x16x32_bf16 v[250:253], a[64:67], v[164:167], v[250:253]\n"
    "ds_read_b128 a[28:31], v241 offset:544\n"
    "v_cvt_pk_bf16_f32 v169, v130, v131\n"
    "v_mfma_f32_32x32x16_bf16 v[32:47], a[60:63], v[164:167], v[32:47]\n"
    "ds_read_b128 a[32:35], v241 offset:768\n"
    "buffer_load_dwordx4 v13, s[28:31], s35 offen offset:1040 nt lds\n"
    "v_add_f32 v10, v10, v128\n"
    "v_exp_f32 v132, v132\n"
    "v_mfma_f32_32x32x16_bf16 v[48:63], a[0:3], v[164:167], v[48:63]\n"
    "ds_read_b128 a[36:39], v241 offset:800\n"
    "v_add_f32 v11, v11, v129\n"
    "v_exp_f32 v133, v133\n"
    "v_exp_f32 v134, v134\n"
    "v_mfma_f32_32x32x16_bf16 v[64:79], a[4:7], v[164:167], v[64:79]\n"
    "s_waitcnt lgkmcnt(2)\n"
    "ds_read_b64_tr_b16 a[40:41], v244 offset:8320\n"
    "ds_read_b64_tr_b16 a[42:43], v244 offset:10400\n"
    "v_cvt_pk_bf16_f32 v170, v132, v133\n"
    "v_add_f32 v10, v10, v130\n"
    "v_exp_f32 v135, v135\n"
    "v_mfma_f32_32x32x16_bf16 v[112:127], a[8:11], v[80:83], v[144:159]\n"
    "ds_read_b64_tr_b16 a[44:45], v244 offset:8576\n"
    "ds_read_b64_tr_b16 a[46:47], v244 offset:10656\n"
    "v_cvt_pk_bf16_f32 v171, v134, v135\n"
    "v_add_f32 v11, v11, v131\n"
    "v_exp_f32 v136, v136\n"
    "v_mfma_f32_32x32x16_bf16 v[112:127], a[12:15], v[84:87], v[112:127]\n"
    "ds_read_b64_tr_b16 a[48:49], v244 offset:8832\n"
    "ds_read_b64_tr_b16 a[50:51], v244 offset:10912\n"
    "buffer_load_dwordx4 v14, s[28:31], s35 offen offset:2080 nt lds\n"
    "v_exp_f32 v137, v137\n"
    "v_mfma_f32_32x32x16_bf16 v[112:127], a[16:19], v[88:91], v[112:127]\n"
    "ds_read_b64_tr_b16 a[52:53], v244 offset:9088\n"
    "ds_read_b64_tr_b16 a[54:55], v244 offset:11168\n"
    "v_cvt_pk_bf16_f32 v172, v136, v137\n"
    "v_add_f32 v10, v10, v132\n"
    "v_exp_f32 v138, v138\n"
    "v_mfma_f32_32x32x16_bf16 v[112:127], a[20:23], v[92:95], v[112:127]\n"
    "ds_read_b64_tr_b16 a[56:57], v244 offset:12480\n"
    "ds_read_b64_tr_b16 a[58:59], v244 offset:14560\n"
    "v_exp_f32 v139, v139\n"
    "v_exp_f32 v140, v140\n"
    "v_mfma_f32_32x32x16_bf16 v[112:127], a[24:27], v[96:99], v[112:127]\n"
    "ds_read_b64_tr_b16 a[60:61], v244 offset:12736\n"
    "ds_read_b64_tr_b16 a[62:63], v244 offset:14816\n"
    "buffer_load_dwordx4 v15, s[28:31], s35 offen offset:3120 nt lds\n"
    "v_cvt_pk_bf16_f32 v173, v138, v139\n"
    "v_add_f32 v11, v11, v133\n"
    "v_mfma_f32_32x32x16_bf16 v[112:127], a[28:31], v[100:103], v[112:127]\n"
    "s_waitcnt lgkmcnt(4)\n"
    "ds_read_b64_tr_b16 a[0:1], v244 offset:12992\n"
    "ds_read_b64_tr_b16 a[2:3], v244 offset:15072\n"
    "v_exp_f32 v141, v141\n"
    "v_exp_f32 v142, v142\n"
    "v_mfma_f32_32x32x16_bf16 v[112:127], a[32:35], v[104:107], v[112:127]\n"
    "s_waitcnt vmcnt(12)\n"
    "ds_read_b64_tr_b16 a[4:5], v244 offset:13248\n"
    "ds_read_b64_tr_b16 a[6:7], v244 offset:15328\n"
    "v_cvt_pk_bf16_f32 v174, v140, v141\n"
    "v_add_f32 v10, v10, v134\n"
    "v_exp_f32 v143, v143\n"
    "v_mfma_f32_32x32x16_bf16 v[112:127], a[36:39], v[108:111], v[112:127]\n"
    "s_barrier\n"
    "ds_read_b128 a[8:11], v241 offset:16640\n"
    "v_cvt_pk_bf16_f32 v175, v142, v143\n"
    "v_mfma_f32_32x32x16_bf16 v[16:31], a[40:43], v[168:171], v[16:31]\n"
    "s_add_u32 m0, s48, 141440\n"
    "ds_read_b128 a[12:15], v241 offset:16672\n"
    "v_add_f32 v11, v11, v135\n"
    "v_add_f32 v10, v10, v136\n"
    "v_add_f32 v11, v11, v137\n"
    "v_add_f32 v10, v10, v138\n"
    "v_add_f32 v11, v11, v139\n"
    "v_mfma_f32_32x32x16_bf16 v[32:47], a[44:47], v[168:171], v[32:47]\n"
    "s_add_u32 s35, s35, 2048\n"
    "ds_read_b128 a[16:19], v241 offset:16896\n"
    "buffer_load_dwordx4 v12, s[28:31], s35 offen offset:0 nt lds\n"
    "v_add_f32 v10, v10, v140\n"
    "v_add_f32 v11, v11, v141\n"
    "v_add_f32 v10, v10, v142\n"
    "v_mfma_f32_32x32x16_bf16 v[48:63], a[48:51], v[168:171], v[48:63]\n"
    "ds_read_b128 a[20:23], v241 offset:16928\n"
    "v_add_f32 v11, v11, v143\n"
    "v_exp_f32 v112, v112\n"
    "v_exp_f32 v113, v113\n"
    "v_mfma_f32_32x32x16_bf16 v[64:79], a[52:55], v[168:171], v[64:79]\n"
    "s_waitcnt lgkmcnt(2)\n"
    "ds_read_b128 a[24:27], v241 offset:17152\n"
    "v_cvt_pk_bf16_f32 v176, v112, v113\n"
    "v_exp_f32 v114, v114\n"
    "v_exp_f32 v115, v115\n"
    "v_mfma_f32_32x32x16_bf16 v[16:31], a[56:59], v[172:175], v[16:31]\n"
    "ds_read_b128 a[28:31], v241 offset:17184\n"
    "buffer_load_dwordx4 v13, s[28:31], s35 offen offset:1040 nt lds\n"
    "v_cvt_pk_bf16_f32 v177, v114, v115\n"
    "v_add_f32 v10, v10, v112\n"
    "v_mfma_f32_32x32x16_bf16 v[32:47], a[60:63], v[172:175], v[32:47]\n"
    "ds_read_b128 a[32:35], v241 offset:17408\n"
    "v_add_f32 v11, v11, v113\n"
    "v_exp_f32 v116, v116\n"
    "v_exp_f32 v117, v117\n"
    "v_mfma_f32_32x32x16_bf16 v[48:63], a[0:3], v[172:175], v[48:63]\n"
    "ds_read_b128 a[36:39], v241 offset:17440\n"
    "v_cvt_pk_bf16_f32 v178, v116, v117\n"
    "v_exp_f32 v118, v118\n"
    "v_exp_f32 v119, v119\n"
    "v_mfma_f32_32x32x16_bf16 v[64:79], a[4:7], v[172:175], v[64:79]\n"
    "ds_read_b64_tr_b16 a[40:41], v244 offset:24960\n"
    "ds_read_b64_tr_b16 a[42:43], v244 offset:27040\n"
    "v_cvt_pk_bf16_f32 v179, v118, v119\n"
    "v_add_f32 v10, v10, v114\n"
    "v_exp_f32 v120, v120\n"
    "v_mfma_f32_32x32x16_bf16 v[128:143], a[8:11], v[80:83], v[144:159]\n"
    "ds_read_b64_tr_b16 a[44:45], v244 offset:25216\n"
    "ds_read_b64_tr_b16 a[46:47], v244 offset:27296\n"
    "buffer_load_dwordx4 v14, s[28:31], s35 offen offset:2080 nt lds\n"
    "v_exp_f32 v121, v121\n"
    "v_mfma_f32_32x32x16_bf16 v[128:143], a[12:15], v[84:87], v[128:143]\n"
    "s_waitcnt lgkmcnt(4)\n"
    "ds_read_b64_tr_b16 a[48:49], v244 offset:25472\n"
    "ds_read_b64_tr_b16 a[50:51], v244 offset:27552\n"
    "v_cvt_pk_bf16_f32 v180, v120, v121\n"
    "v_add_f32 v11, v11, v115\n"
    "v_exp_f32 v122, v122\n"
    "v_mfma_f32_32x32x16_bf16 v[128:143], a[16:19], v[88:91], v[128:143]\n"
    "ds_read_b64_tr_b16 a[52:53], v244 offset:25728\n"
    "ds_read_b64_tr_b16 a[54:55], v244 offset:27808\n"
    "v_exp_f32 v123, v123\n"
    "v_exp_f32 v124, v124\n"
    "v_mfma_f32_32x32x16_bf16 v[128:143], a[20:23], v[92:95], v[128:143]\n"
    "ds_read_b64_tr_b16 a[56:57], v244 offset:29120\n"
    "ds_read_b64_tr_b16 a[58:59], v244 offset:31200\n"
    "v_cvt_pk_bf16_f32 v181, v122, v123\n"
    "v_exp_f32 v125, v125\n"
    "v_mfma_f32_32x32x16_bf16 v[128:143], a[24:27], v[96:99], v[128:143]\n"
    "ds_read_b64_tr_b16 a[60:61], v244 offset:29376\n"
    "ds_read_b64_tr_b16 a[62:63], v244 offset:31456\n"
    "buffer_load_dwordx4 v15, s[28:31], s35 offen offset:3120 nt lds\n"
    "v_cvt_pk_bf16_f32 v182, v124, v125\n"
    "v_add_f32 v10, v10, v116\n"
    "v_mfma_f32_32x32x16_bf16 v[128:143], a[28:31], v[100:103], v[128:143]\n"
    "s_add_u32 m0, s48, 8320\n"
    "ds_read_b64_tr_b16 a[0:1], v244 offset:29632\n"
    "ds_read_b64_tr_b16 a[2:3], v244 offset:31712\n"
    "v_exp_f32 v126, v126\n"
    "v_exp_f32 v127, v127\n"
    "v_mfma_f32_32x32x16_bf16 v[128:143], a[32:35], v[104:107], v[128:143]\n"
    "s_waitcnt vmcnt(12) lgkmcnt(4)\n"
    "ds_read_b64_tr_b16 a[4:5], v244 offset:29888\n"
    "ds_read_b64_tr_b16 a[6:7], v244 offset:31968\n"
    "v_cvt_pk_bf16_f32 v183, v126, v127\n"
    "v_add_f32 v11, v11, v117\n"
    "v_add_f32 v10, v10, v118\n"
    "v_mfma_f32_32x32x16_bf16 v[128:143], a[36:39], v[108:111], v[128:143]\n"
    "s_barrier\n"
    "ds_read_b128 a[8:11], v241 offset:33280\n"
    "v_add_f32 v11, v11, v119\n"
    "v_mfma_f32_32x32x16_bf16 v[16:31], a[40:43], v[176:179], v[16:31]\n"
    "s_add_u32 s35, s35, 2048\n"
    "ds_read_b128 a[12:15], v241 offset:33312\n"
    "v_add_f32 v10, v10, v120\n"
    "v_add_f32 v11, v11, v121\n"
    "v_add_f32 v10, v10, v122\n"
    "v_add_f32 v11, v11, v123\n"
    "v_add_f32 v10, v10, v124\n"
    "v_mfma_f32_32x32x16_bf16 v[32:47], a[44:47], v[176:179], v[32:47]\n"
    "ds_read_b128 a[16:19], v241 offset:33536\n"
    "buffer_load_dwordx4 v12, s[28:31], s35 offen offset:0 nt lds\n"
    "v_add_f32 v11, v11, v125\n"
    "v_add_f32 v10, v10, v126\n"
    "v_mfma_f32_32x32x16_bf16 v[48:63], a[48:51], v[176:179], v[48:63]\n"
    "ds_read_b128 a[20:23], v241 offset:33568\n"
    "v_add_f32 v11, v11, v127\n"
    "v_exp_f32 v128, v128\n"
    "v_exp_f32 v129, v129\n"
    "v_mfma_f32_32x32x16_bf16 v[64:79], a[52:55], v[176:179], v[64:79]\n"
    "ds_read_b128 a[24:27], v241 offset:33792\n"
    "v_cvt_pk_bf16_f32 v184, v128, v129\n"
    "v_exp_f32 v130, v130\n"
    "v_exp_f32 v131, v131\n"
    "v_mfma_f32_32x32x16_bf16 v[16:31], a[56:59], v[180:183], v[16:31]\n"
    "s_waitcnt lgkmcnt(2)\n"
    "ds_read_b128 a[28:31], v241 offset:33824\n"
    "buffer_load_dwordx4 v13, s[28:31], s35 offen offset:1040 nt lds\n"
    "v_cvt_pk_bf16_f32 v185, v130, v131\n"
    "v_add_f32 v10, v10, v128\n"
    "v_mfma_f32_32x32x16_bf16 v[32:47], a[60:63], v[180:183], v[32:47]\n"
    "ds_read_b128 a[32:35], v241 offset:34048\n"
    "v_add_f32 v11, v11, v129\n"
    "v_exp_f32 v132, v132\n"
    "v_exp_f32 v133, v133\n"
    "v_mfma_f32_32x32x16_bf16 v[48:63], a[0:3], v[180:183], v[48:63]\n"
    "ds_read_b128 a[36:39], v241 offset:34080\n"
    "v_cvt_pk_bf16_f32 v186, v132, v133\n"
    "v_exp_f32 v134, v134\n"
    "v_exp_f32 v135, v135\n"
    "v_mfma_f32_32x32x16_bf16 v[64:79], a[4:7], v[180:183], v[64:79]\n"
    ".Lq_tp0_%=:\n"
    "s_cbranch_execz .Lq_tret_%=\n"
    "ds_read_b64_tr_b16 a[40:41], v244 offset:41600\n"
    "ds_read_b64_tr_b16 a[42:43], v244 offset:43680\n"
    "buffer_load_dwordx4 v14, s[28:31], s35 offen offset:2080 nt lds\n"
    "v_cvt_pk_bf16_f32 v187, v134, v135\n"
    "v_add_f32 v10, v10, v130\n"
    "v_mfma_f32_32x32x16_bf16 v[112:127], a[8:11], v[80:83], v[144:159]\n"
    "ds_read_b64_tr_b16 a[44:45], v244 offset:41856\n"
    "ds_read_b64_tr_b16 a[46:47], v244 offset:43936\n"
    "v_exp_f32 v136, v136\n"
    "v_exp_f32 v137, v137\n"
    "v_mfma_f32_32x32x16_bf16 v[112:127], a[12:15], v[84:87], v[112:127]\n"
    "ds_read_b64_tr_b16 a[48:49], v244 offset:42112\n"
    "ds_read_b64_tr_b16 a[50:51], v244 offset:44192\n"
    "v_cvt_pk_bf16_f32 v188, v136, v137\n"
    "v_add_f32 v11, v11, v131\n"
    "v_exp_f32 v138, v138\n"
    "v_mfma_f32_32x32x16_bf16 v[112:127], a[16:19], v[88:91], v[112:127]\n"
    "s_waitcnt lgkmcnt(4)\n"
    "ds_read_b64_tr_b16 a[52:53], v244 offset:42368\n"
    "ds_read_b64_tr_b16 a[54:55], v244 offset:44448\n"
    "buffer_load_dwordx4 v15, s[28:31], s35 offen offset:3120 nt lds\n"
    "v_exp_f32 v139, v139\n"
    "v_mfma_f32_32x32x16_bf16 v[112:127], a[20:23], v[92:95], v[112:127]\n"
    "s_add_u32 m0, s48, 24960\n"
    "ds_read_b64_tr_b16 a[56:57], v244 offset:45760\n"
    "ds_read_b64_tr_b16 a[58:59], v244 offset:47840\n"
    "v_cvt_pk_bf16_f32 v189, v138, v139\n"
    "v_add_f32 v10, v10, v132\n"
    "v_exp_f32 v140, v140\n"
    "v_mfma_f32_32x32x16_bf16 v[112:127], a[24:27], v[96:99], v[112:127]\n"
    "s_add_u32 s35, s35, 2048\n"
    "ds_read_b64_tr_b16 a[60:61], v244 offset:46016\n"
    "ds_read_b64_tr_b16 a[62:63], v244 offset:48096\n"
    "v_exp_f32 v141, v141\n"
    "v_exp_f32 v142, v142\n"
    "v_mfma_f32_32x32x16_bf16 v[112:127], a[28:31], v[100:103], v[112:127]\n"
    "ds_read_b64_tr_b16 a[0:1], v244 offset:46272\n"
    "ds_read_b64_tr_b16 a[2:3], v244 offset:48352\n"
    "buffer_load_dwordx4 v12, s[28:31], s35 offen offset:0 nt lds\n"
    "v_cvt_pk_bf16_f32 v190, v140, v141\n"
    "v_add_f32 v11, v11, v133\n"
    "v_mfma_f32_32x32x16_bf16 v[112:127], a[32:35], v[104:107], v[112:127]\n"
    "s_waitcnt vmcnt(13)\n"
    "ds_read_b64_tr_b16 a[4:5], v244 offset:46528\n"
    "ds_read_b64_tr_b16 a[6:7], v244 offset:48608\n"
    "v_exp_f32 v143, v143\n"
    "v_add_f32 v10, v10, v134\n"
    "v_cvt_pk_bf16_f32 v191, v142, v143\n"
    "v_mfma_f32_32x32x16_bf16 v[112:127], a[36:39], v[108:111], v[112:127]\n"
    "s_barrier\n"
    "ds_read_b128 a[8:11], v241 offset:49920\n"
    "v_add_f32 v11, v11, v135\n"
    "v_mfma_f32_32x32x16_bf16 v[16:31], a[40:43], v[184:187], v[16:31]\n"
    "s_waitcnt lgkmcnt(3)\n"
    "ds_read_b128 a[12:15], v241 offset:49952\n"
    "v_add_f32 v10, v10, v136\n"
    "v_add_f32 v11, v11, v137\n"
    "v_add_f32 v10, v10, v138\n"
    "v_add_f32 v11, v11, v139\n"
    "v_add_f32 v10, v10, v140\n"
    "v_mfma_f32_32x32x16_bf16 v[32:47], a[44:47], v[184:187], v[32:47]\n"
    "ds_read_b128 a[16:19], v241 offset:50176\n"
    "buffer_load_dwordx4 v13, s[28:31], s35 offen offset:1040 nt lds\n"
    "v_add_f32 v11, v11, v141\n"
    "v_add_f32 v10, v10, v142\n"
    "v_mfma_f32_32x32x16_bf16 v[48:63], a[48:51], v[184:187], v[48:63]\n"
    "ds_read_b128 a[20:23], v241 offset:50208\n"
    "v_add_f32 v11, v11, v143\n"
    "v_exp_f32 v112, v112\n"
    "v_exp_f32 v113, v113\n"
    "v_mfma_f32_32x32x16_bf16 v[64:79], a[52:55], v[184:187], v[64:79]\n"
    "ds_read_b128 a[24:27], v241 offset:50432\n"
    "v_cvt_pk_bf16_f32 v192, v112, v113\n"
    "v_exp_f32 v114, v114\n"
    "v_exp_f32 v115, v115\n"
    "v_mfma_f32_32x32x16_bf16 v[16:31], a[56:59], v[188:191], v[16:31]\n"
    "ds_read_b128 a[28:31], v241 offset:50464\n"
    "buffer_load_dwordx4 v14, s[28:31], s35 offen offset:2080 nt lds\n"
    "v_cvt_pk_bf16_f32 v193, v114, v115\n"
    "v_add_f32 v10, v10, v112\n"
    "v_mfma_f32_32x32x16_bf16 v[32:47], a[60:63], v[188:191], v[32:47]\n"
    "ds_read_b128 a[32:35], v241 offset:50688\n"
    "v_add_f32 v11, v11, v113\n"
    "v_exp_f32 v116, v116\n"
    "v_exp_f32 v117, v117\n"
    "v_mfma_f32_32x32x16_bf16 v[48:63], a[0:3], v[188:191], v[48:63]\n"
    "s_waitcnt lgkmcnt(2)\n"
    "ds_read_b128 a[36:39], v241 offset:50720\n"
    "v_cvt_pk_bf16_f32 v194, v116, v117\n"
    "v_exp_f32 v118, v118\n"
    "v_exp_f32 v119, v119\n"
    "v_mfma_f32_32x32x16_bf16 v[64:79], a[4:7], v[188:191], v[64:79]\n"
    "ds_read_b64_tr_b16 a[40:41], v245 offset:0\n"
    "ds_read_b64_tr_b16 a[42:43], v245 offset:2080\n"
    "buffer_load_dwordx4 v15, s[28:31], s35 offen offset:3120 nt lds\n"
    "v_cvt_pk_bf16_f32 v195, v118, v119\n"
    "v_add_f32 v10, v10, v114\n"
    "v_mfma_f32_32x32x16_bf16 v[128:143], a[8:11], v[80:83], v[144:159]\n"
    "s_add_u32 m0, s48, 41600\n"
    "ds_read_b64_tr_b16 a[44:45], v245 offset:256\n"
    "ds_read_b64_tr_b16 a[46:47], v245 offset:2336\n"
    "v_exp_f32 v120, v120\n"
    "v_exp_f32 v121, v121\n"
    "v_mfma_f32_32x32x16_bf16 v[128:143], a[12:15], v[84:87], v[128:143]\n"
    "s_add_u32 s35, s35, 2048\n"
    "ds_read_b64_tr_b16 a[48:49], v245 offset:512\n"
    "ds_read_b64_tr_b16 a[50:51], v245 offset:2592\n"
    "v_cvt_pk_bf16_f32 v196, v120, v121\n"
    "v_add_f32 v11, v11, v115\n"
    "v_exp_f32 v122, v122\n"
    "v_mfma_f32_32x32x16_bf16 v[128:143], a[16:19], v[88:91], v[128:143]\n"
    "ds_read_b64_tr_b16 a[52:53], v245 offset:768\n"
    "ds_read_b64_tr_b16 a[54:55], v245 offset:2848\n"
    "buffer_load_dwordx4 v12, s[28:31], s35 offen offset:0 nt lds\n"
    "v_exp_f32 v123, v123\n"
    "v_mfma_f32_32x32x16_bf16 v[128:143], a[20:23], v[92:95], v[128:143]\n"
    "ds_read_b64_tr_b16 a[56:57], v245 offset:4160\n"
    "ds_read_b64_tr_b16 a[58:59], v245 offset:6240\n"
    "v_cvt_pk_bf16_f32 v197, v122, v123\n"
    "v_add_f32 v10, v10, v116\n"
    "v_exp_f32 v124, v124\n"
    "v_mfma_f32_32x32x16_bf16 v[128:143], a[24:27], v[96:99], v[128:143]\n"
    "s_waitcnt lgkmcnt(4)\n"
    "ds_read_b64_tr_b16 a[60:61], v245 offset:4416\n"
    "ds_read_b64_tr_b16 a[62:63], v245 offset:6496\n"
    "v_exp_f32 v125, v125\n"
    "v_exp_f32 v126, v126\n"
    "v_mfma_f32_32x32x16_bf16 v[128:143], a[28:31], v[100:103], v[128:143]\n"
    "ds_read_b64_tr_b16 a[0:1], v245 offset:4672\n"
    "ds_read_b64_tr_b16 a[2:3], v245 offset:6752\n"
    "buffer_load_dwordx4 v13, s[28:31], s35 offen offset:1040 nt lds\n"
    "v_cvt_pk_bf16_f32 v198, v124, v125\n"
    "v_add_f32 v11, v11, v117\n"
    "v_mfma_f32_32x32x16_bf16 v[128:143], a[32:35], v[104:107], v[128:143]\n"
    "s_waitcnt vmcnt(14)\n"
    "ds_read_b64_tr_b16 a[4:5], v245 offset:4928\n"
    "ds_read_b64_tr_b16 a[6:7], v245 offset:7008\n"
    "v_exp_f32 v127, v127\n"
    "v_add_f32 v10, v10, v118\n"
    "v_cvt_pk_bf16_f32 v199, v126, v127\n"
    "v_mfma_f32_32x32x16_bf16 v[128:143], a[36:39], v[108:111], v[128:143]\n"
    "s_barrier\n"
    "ds_read_b128 a[8:11], v242 offset:8320\n"
    "v_add_f32 v11, v11, v119\n"
    "v_mfma_f32_32x32x16_bf16 v[16:31], a[40:43], v[192:195], v[16:31]\n"
    "ds_read_b128 a[12:15], v242 offset:8352\n"
    "v_add_f32 v10, v10, v120\n"
    "v_add_f32 v11, v11, v121\n"
    "v_add_f32 v10, v10, v122\n"
    "v_add_f32 v11, v11, v123\n"
    "v_add_f32 v10, v10, v124\n"
    "v_mfma_f32_32x32x16_bf16 v[32:47], a[44:47], v[192:195], v[32:47]\n"
    "ds_read_b128 a[16:19], v242 offset:8576\n"
    "buffer_load_dwordx4 v14, s[28:31], s35 offen offset:2080 nt lds\n"
    "v_add_f32 v11, v11, v125\n"
    "v_add_f32 v10, v10, v126\n"
    "v_mfma_f32_32x32x16_bf16 v[48:63], a[48:51], v[192:195], v[48:63]\n"
    "s_waitcnt lgkmcnt(2)\n"
    "ds_read_b128 a[20:23], v242 offset:8608\n"
    "v_add_f32 v11, v11, v127\n"
    "v_exp_f32 v128, v128\n"
    "v_exp_f32 v129, v129\n"
    "v_mfma_f32_32x32x16_bf16 v[64:79], a[52:55], v[192:195], v[64:79]\n"
    "ds_read_b128 a[24:27], v242 offset:8832\n"
    "v_cvt_pk_bf16_f32 v200, v128, v129\n"
    "v_exp_f32 v130, v130\n"
    "v_exp_f32 v131, v131\n"
    "v_mfma_f32_32x32x16_bf16 v[16:31], a[56:59], v[196:199], v[16:31]\n"
    "ds_read_b128 a[28:31], v242 offset:8864\n"
    "buffer_load_dwordx4 v15, s[28:31], s35 offen offset:3120 nt lds\n"
    "v_cvt_pk_bf16_f32 v201, v130, v131\n"
    "v_add_f32 v10, v10, v128\n"
    "v_mfma_f32_32x32x16_bf16 v[32:47], a[60:63], v[196:199], v[32:47]\n"
    "s_add_u32 m0, s48, 58240\n"
    "ds_read_b128 a[32:35], v242 offset:9088\n"
    "v_add_f32 v11, v11, v129\n"
    "v_exp_f32 v132, v132\n"
    "v_exp_f32 v133, v133\n"
    "v_mfma_f32_32x32x16_bf16 v[48:63], a[0:3], v[196:199], v[48:63]\n"
    "s_add_u32 s35, s35, 2048\n"
    "ds_read_b128 a[36:39], v242 offset:9120\n"
    "v_cvt_pk_bf16_f32 v202, v132, v133\n"
    "v_exp_f32 v134, v134\n"
    "v_exp_f32 v135, v135\n"
    "v_mfma_f32_32x32x16_bf16 v[64:79], a[4:7], v[196:199], v[64:79]\n"
    "ds_read_b64_tr_b16 a[40:41], v245 offset:16640\n"
    "ds_read_b64_tr_b16 a[42:43], v245 offset:18720\n"
    "buffer_load_dwordx4 v12, s[28:31], s35 offen offset:0 lds\n"
    "v_cvt_pk_bf16_f32 v203, v134, v135\n"
    "v_add_f32 v10, v10, v130\n"
    "v_mfma_f32_32x32x16_bf16 v[112:127], a[8:11], v[80:83], v[144:159]\n"
    "s_waitcnt lgkmcnt(3)\n"
    "ds_read_b64_tr_b16 a[44:45], v245 offset:16896\n"
    "ds_read_b64_tr_b16 a[46:47], v245 offset:18976\n"
    "v_exp_f32 v136, v136\n"
    "v_exp_f32 v137, v137\n"
    "v_mfma_f32_32x32x16_bf16 v[112:127], a[12:15], v[84:87], v[112:127]\n"
    "ds_read_b64_tr_b16 a[48:49], v245 offset:17152\n"
    "ds_read_b64_tr_b16 a[50:51], v245 offset:19232\n"
    "v_cvt_pk_bf16_f32 v204, v136, v137\n"
    "v_add_f32 v11, v11, v131\n"
    "v_exp_f32 v138, v138\n"
    "v_mfma_f32_32x32x16_bf16 v[112:127], a[16:19], v[88:91], v[112:127]\n"
    "ds_read_b64_tr_b16 a[52:53], v245 offset:17408\n"
    "ds_read_b64_tr_b16 a[54:55], v245 offset:19488\n"
    "buffer_load_dwordx4 v13, s[28:31], s35 offen offset:1040 lds\n"
    "v_exp_f32 v139, v139\n"
    "v_mfma_f32_32x32x16_bf16 v[112:127], a[20:23], v[92:95], v[112:127]\n"
    "ds_read_b64_tr_b16 a[56:57], v245 offset:20800\n"
    "ds_read_b64_tr_b16 a[58:59], v245 offset:22880\n"
    "v_cvt_pk_bf16_f32 v205, v138, v139\n"
    "v_add_f32 v10, v10, v132\n"
    "v_exp_f32 v140, v140\n"
    "v_mfma_f32_32x32x16_bf16 v[112:127], a[24:27], v[96:99], v[112:127]\n"
    "ds_read_b64_tr_b16 a[60:61], v245 offset:21056\n"
    "ds_read_b64_tr_b16 a[62:63], v245 offset:23136\n"
    "v_exp_f32 v141, v141\n"
    "v_exp_f32 v142, v142\n"
    "v_mfma_f32_32x32x16_bf16 v[112:127], a[28:31], v[100:103], v[112:127]\n"
    "ds_read_b64_tr_b16 a[0:1], v245 offset:21312\n"
    "ds_read_b64_tr_b16 a[2:3], v245 offset:23392\n"
    "buffer_load_dwordx4 v14, s[28:31], s35 offen offset:2080 lds\n"
    "v_cvt_pk_bf16_f32 v206, v140, v141\n"
    "v_add_f32 v11, v11, v133\n"
    "v_mfma_f32_32x32x16_bf16 v[112:127], a[32:35], v[104:107], v[112:127]\n"
    "s_waitcnt vmcnt(15) lgkmcnt(4)\n"
    "ds_read_b64_tr_b16 a[4:5], v245 offset:21568\n"
    "ds_read_b64_tr_b16 a[6:7], v245 offset:23648\n"
    "v_exp_f32 v143, v143\n"
    "v_add_f32 v10, v10, v134\n"
    "v_cvt_pk_bf16_f32 v207, v142, v143\n"
    "v_mfma_f32_32x32x16_bf16 v[112:127], a[36:39], v[108:111], v[112:127]\n"
    "s_barrier\n"
    "ds_read_b128 a[8:11], v242 offset:24960\n"
    "v_add_f32 v11, v11, v135\n"
    "v_mfma_f32_32x32x16_bf16 v[16:31], a[40:43], v[200:203], v[16:31]\n"
    "ds_read_b128 a[12:15], v242 offset:24992\n"
    "v_add_f32 v10, v10, v136\n"
    "v_add_f32 v11, v11, v137\n"
    "v_add_f32 v10, v10, v138\n"
    "v_add_f32 v11, v11, v139\n"
    "v_add_f32 v10, v10, v140\n"
    "v_mfma_f32_32x32x16_bf16 v[32:47], a[44:47], v[200:203], v[32:47]\n"
    "ds_read_b128 a[16:19], v242 offset:25216\n"
    "buffer_load_dwordx4 v15, s[28:31], s35 offen offset:3120 lds\n"
    "v_add_f32 v11, v11, v141\n"
    "v_add_f32 v10, v10, v142\n"
    "v_mfma_f32_32x32x16_bf16 v[48:63], a[48:51], v[200:203], v[48:63]\n"
    "s_add_u32 m0, s48, 74880\n"
    "ds_read_b128 a[20:23], v242 offset:25248\n"
    "v_add_f32 v11, v11, v143\n"
    "v_exp_f32 v112, v112\n"
    "v_exp_f32 v113, v113\n"
    "v_mfma_f32_32x32x16_bf16 v[64:79], a[52:55], v[200:203], v[64:79]\n"
    "s_add_u32 s35, s35, 2048\n"
    "ds_read_b128 a[24:27], v242 offset:25472\n"
    "v_cvt_pk_bf16_f32 v208, v112, v113\n"
    "v_exp_f32 v114, v114\n"
    "v_exp_f32 v115, v115\n"
    "v_mfma_f32_32x32x16_bf16 v[16:31], a[56:59], v[204:207], v[16:31]\n"
    "s_waitcnt lgkmcnt(2)\n"
    "ds_read_b128 a[28:31], v242 offset:25504\n"
    "buffer_load_dwordx4 v12, s[28:31], s35 offen offset:0 lds\n"
    "v_cvt_pk_bf16_f32 v209, v114, v115\n"
    "v_add_f32 v10, v10, v112\n"
    "v_mfma_f32_32x32x16_bf16 v[32:47], a[60:63], v[204:207], v[32:47]\n"
    "ds_read_b128 a[32:35], v242 offset:25728\n"
    "v_exp_f32 v116, v116\n"
    "v_exp_f32 v117, v117\n"
    "v_add_f32 v11, v11, v113\n"
    "v_mfma_f32_32x32x16_bf16 v[48:63], a[0:3], v[204:207], v[48:63]\n"
    "ds_read_b128 a[36:39], v242 offset:25760\n"
    "v_cvt_pk_bf16_f32 v210, v116, v117\n"
    "v_exp_f32 v118, v118\n"
    "v_exp_f32 v119, v119\n"
    "v_mfma_f32_32x32x16_bf16 v[64:79], a[4:7], v[204:207], v[64:79]\n"
    "ds_read_b64_tr_b16 a[40:41], v245 offset:33280\n"
    "ds_read_b64_tr_b16 a[42:43], v245 offset:35360\n"
    "buffer_load_dwordx4 v13, s[28:31], s35 offen offset:1040 lds\n"
    "v_cvt_pk_bf16_f32 v211, v118, v119\n"
    "v_add_f32 v10, v10, v114\n"
    "v_mfma_f32_32x32x16_bf16 v[128:143], a[8:11], v[80:83], v[144:159]\n"
    "ds_read_b64_tr_b16 a[44:45], v245 offset:33536\n"
    "ds_read_b64_tr_b16 a[46:47], v245 offset:35616\n"
    "v_exp_f32 v120, v120\n"
    "v_exp_f32 v121, v121\n"
    "v_mfma_f32_32x32x16_bf16 v[128:143], a[12:15], v[84:87], v[128:143]\n"
    "ds_read_b64_tr_b16 a[48:49], v245 offset:33792\n"
    "ds_read_b64_tr_b16 a[50:51], v245 offset:35872\n"
    "v_cvt_pk_bf16_f32 v212, v120, v121\n"
    "v_exp_f32 v122, v122\n"
    "v_add_f32 v11, v11, v115\n"
    "v_mfma_f32_32x32x16_bf16 v[128:143], a[16:19], v[88:91], v[128:143]\n"
    "s_waitcnt lgkmcnt(4)\n"
    "ds_read_b64_tr_b16 a[52:53], v245 offset:34048\n"
    "ds_read_b64_tr_b16 a[54:55], v245 offset:36128\n"
    "buffer_load_dwordx4 v14, s[28:31], s35 offen offset:2080 lds\n"
    "v_exp_f32 v123, v123\n"
    "v_mfma_f32_32x32x16_bf16 v[128:143], a[20:23], v[92:95], v[128:143]\n"
    "ds_read_b64_tr_b16 a[56:57], v245 offset:37440\n"
    "ds_read_b64_tr_b16 a[58:59], v245 offset:39520\n"
    "v_cvt_pk_bf16_f32 v213, v122, v123\n"
    "v_exp_f32 v124, v124\n"
    "v_add_f32 v10, v10, v116\n"
    "v_mfma_f32_32x32x16_bf16 v[128:143], a[24:27], v[96:99], v[128:143]\n"
    "ds_read_b64_tr_b16 a[60:61], v245 offset:37696\n"
    "ds_read_b64_tr_b16 a[62:63], v245 offset:39776\n"
    "v_exp_f32 v125, v125\n"
    "v_exp_f32 v126, v126\n"
    "v_mfma_f32_32x32x16_bf16 v[128:143], a[28:31], v[100:103], v[128:143]\n"
    "ds_read_b64_tr_b16 a[0:1], v245 offset:37952\n"
    "ds_read_b64_tr_b16 a[2:3], v245 offset:40032\n"
    "buffer_load_dwordx4 v15, s[28:31], s35 offen offset:3120 lds\n"
    "v_cvt_pk_bf16_f32 v214, v124, v125\n"
    "v_add_f32 v11, v11, v117\n"
    "v_mfma_f32_32x32x16_bf16 v[128:143], a[32:35], v[104:107], v[128:143]\n"
    "s_waitcnt vmcnt(16)\n"
    "ds_read_b64_tr_b16 a[4:5], v245 offset:38208\n"
    "ds_read_b64_tr_b16 a[6:7], v245 offset:40288\n"
    "v_exp_f32 v127, v127\n"
    "v_add_f32 v10, v10, v118\n"
    "v_cvt_pk_bf16_f32 v215, v126, v127\n"
    "v_mfma_f32_32x32x16_bf16 v[128:143], a[36:39], v[108:111], v[128:143]\n"
    "s_barrier\n"
    "ds_read_b128 a[8:11], v240 offset:8320\n"
    "v_add_f32 v11, v11, v119\n"
    "v_mfma_f32_32x32x16_bf16 v[16:31], a[40:43], v[208:211], v[16:31]\n"
    "s_waitcnt lgkmcnt(3)\n"
    "ds_read_b128 a[12:15], v240 offset:8352\n"
    "v_add_f32 v10, v10, v120\n"
    "v_add_f32 v11, v11, v121\n"
    "v_add_f32 v10, v10, v122\n"
    "v_add_f32 v11, v11, v123\n"
    "v_add_f32 v10, v10, v124\n"
    "v_mfma_f32_32x32x16_bf16 v[32:47], a[44:47], v[208:211], v[32:47]\n"
    "s_add_u32 m0, s48, 91520\n"
    "s_add_u32 s35, s35, 2048\n"
    "ds_read_b128 a[16:19], v240 offset:8576\n"
    "buffer_load_dwordx4 v12, s[28:31], s35 offen offset:0 lds\n"
    "v_add_f32 v11, v11, v125\n"
    "v_mfma_f32_32x32x16_bf16 v[48:63], a[48:51], v[208:211], v[48:63]\n"
    "ds_read_b128 a[20:23], v240 offset:8608\n"
    "v_add_f32 v10, v10, v126\n"
    "v_add_f32 v11, v11, v127\n"
    "v_exp_f32 v128, v128\n"
    "v_mfma_f32_32x32x16_bf16 v[64:79], a[52:55], v[208:211], v[64:79]\n"
    "ds_read_b128 a[24:27], v240 offset:8832\n"
    "v_exp_f32 v129, v129\n"
    "v_exp_f32 v130, v130\n"
    "v_cvt_pk_bf16_f32 v216, v128, v129\n"
    "v_mfma_f32_32x32x16_bf16 v[16:31], a[56:59], v[212:215], v[16:31]\n"
    "ds_read_b128 a[28:31], v240 offset:8864\n"
    "buffer_load_dwordx4 v13, s[28:31], s35 offen offset:1040 lds\n"
    "v_exp_f32 v131, v131\n"
    "v_mfma_f32_32x32x16_bf16 v[32:47], a[60:63], v[212:215], v[32:47]\n"
    "ds_read_b128 a[32:35], v240 offset:9088\n"
    "v_cvt_pk_bf16_f32 v217, v130, v131\n"
    "v_exp_f32 v132, v132\n"
    "v_add_f32 v10, v10, v128\n"
    "v_mfma_f32_32x32x16_bf16 v[48:63], a[0:3], v[212:215], v[48:63]\n"
    "s_waitcnt lgkmcnt(2)\n"
    "ds_read_b128 a[36:39], v240 offset:9120\n"
    "v_exp_f32 v133, v133\n"
    "v_exp_f32 v134, v134\n"
    "v_cvt_pk_bf16_f32 v218, v132, v133\n"
    "v_mfma_f32_32x32x16_bf16 v[64:79], a[4:7], v[212:215], v[64:79]\n"
    "ds_read_b64_tr_b16 a[40:41], v243 offset:16640\n"
    "ds_read_b64_tr_b16 a[42:43], v243 offset:18720\n"
    "buffer_load_dwordx4 v14, s[28:31], s35 offen offset:2080 lds\n"
    "v_exp_f32 v135, v135\n"
    "v_mfma_f32_32x32x16_bf16 v[112:127], a[8:11], v[80:83], v[144:159]\n"
    "s_waitcnt vmcnt(15)\n"
    "ds_read_b64_tr_b16 a[44:45], v243 offset:20800\n"
    "ds_read_b64_tr_b16 a[46:47], v243 offset:22880\n"
    "v_cvt_pk_bf16_f32 v219, v134, v135\n"
    "v_exp_f32 v136, v136\n"
    "v_mfma_f32_32x32x16_bf16 v[112:127], a[12:15], v[84:87], v[112:127]\n"
    "s_barrier\n"
    "ds_read_b128 a[48:51], v240 offset:24960\n"
    "v_add_f32 v11, v11, v129\n"
    "v_mfma_f32_32x32x16_bf16 v[112:127], a[16:19], v[88:91], v[112:127]\n"
    "ds_read_b128 a[52:55], v240 offset:24992\n"
    "v_exp_f32 v137, v137\n"
    "v_exp_f32 v138, v138\n"
    "v_cvt_pk_bf16_f32 v220, v136, v137\n"
    "v_mfma_f32_32x32x16_bf16 v[112:127], a[20:23], v[92:95], v[112:127]\n"
    "ds_read_b128 a[56:59], v240 offset:25216\n"
    "buffer_load_dwordx4 v15, s[28:31], s35 offen offset:3120 lds\n"
    "v_exp_f32 v139, v139\n"
    "v_mfma_f32_32x32x16_bf16 v[112:127], a[24:27], v[96:99], v[112:127]\n"
    "s_waitcnt lgkmcnt(2)\n"
    "ds_read_b128 a[60:63], v240 offset:25248\n"
    "v_cvt_pk_bf16_f32 v221, v138, v139\n"
    "v_exp_f32 v140, v140\n"
    "v_exp_f32 v141, v141\n"
    "v_mfma_f32_32x32x16_bf16 v[112:127], a[28:31], v[100:103], v[112:127]\n"
    "s_add_u32 m0, s48, 108160\n"
    "ds_read_b128 a[0:3], v240 offset:25472\n"
    "v_cvt_pk_bf16_f32 v222, v140, v141\n"
    "v_exp_f32 v142, v142\n"
    "v_exp_f32 v143, v143\n"
    "v_mfma_f32_32x32x16_bf16 v[112:127], a[32:35], v[104:107], v[112:127]\n"
    "s_add_u32 s35, s35, 2048\n"
    "ds_read_b128 a[4:7], v240 offset:25504\n"
    "buffer_load_dwordx4 v12, s[28:31], s35 offen offset:0 lds\n"
    "v_cvt_pk_bf16_f32 v223, v142, v143\n"
    "v_add_f32 v10, v10, v130\n"
    "v_mfma_f32_32x32x16_bf16 v[112:127], a[36:39], v[108:111], v[112:127]\n"
    "ds_read_b128 a[8:11], v240 offset:25728\n"
    "v_add_f32 v11, v11, v131\n"
    "v_add_f32 v10, v10, v132\n"
    "v_add_f32 v11, v11, v133\n"
    "v_add_f32 v10, v10, v134\n"
    "v_mfma_f32_32x32x16_bf16 v[16:31], a[40:43], v[216:219], v[16:31]\n"
    "ds_read_b128 a[12:15], v240 offset:25760\n"
    "v_add_f32 v11, v11, v135\n"
    "v_add_f32 v10, v10, v136\n"
    "v_add_f32 v11, v11, v137\n"
    "v_add_f32 v10, v10, v138\n"
    "v_add_f32 v11, v11, v139\n"
    "v_mfma_f32_32x32x16_bf16 v[16:31], a[44:47], v[220:223], v[16:31]\n"
    "ds_read_b64_tr_b16 a[16:17], v243 offset:33280\n"
    "ds_read_b64_tr_b16 a[18:19], v243 offset:35360\n"
    "buffer_load_dwordx4 v13, s[28:31], s35 offen offset:1040 lds\n"
    "v_add_f32 v10, v10, v140\n"
    "v_add_f32 v11, v11, v141\n"
    "v_add_f32 v10, v10, v142\n"
    "v_add_f32 v11, v11, v143\n"
    "v_mfma_f32_32x32x16_bf16 v[128:143], a[48:51], v[80:83], v[144:159]\n"
    "s_waitcnt lgkmcnt(3)\n"
    "v_exp_f32 v112, v112\n"
    "v_exp_f32 v113, v113\n"
    "v_exp_f32 v114, v114\n"
    "v_mfma_f32_32x32x16_bf16 v[128:143], a[52:55], v[84:87], v[128:143]\n"
    "ds_read_b64_tr_b16 a[24:25], v243 offset:37440\n"
    "ds_read_b64_tr_b16 a[26:27], v243 offset:39520\n"
    "buffer_load_dwordx4 v14, s[28:31], s35 offen offset:2080 lds\n"
    "v_cvt_pk_bf16_f32 v224, v112, v113\n"
    "v_mfma_f32_32x32x16_bf16 v[128:143], a[56:59], v[88:91], v[128:143]\n"
    "s_waitcnt vmcnt(15)\n"
    "v_exp_f32 v115, v115\n"
    "v_exp_f32 v116, v116\n"
    "v_cvt_pk_bf16_f32 v225, v114, v115\n"
    "v_exp_f32 v117, v117\n"
    "v_mfma_f32_32x32x16_bf16 v[128:143], a[60:63], v[92:95], v[128:143]\n"
    "s_barrier\n"
    "ds_read_b128 a[32:35], v240 offset:41600\n"
    "v_cvt_pk_bf16_f32 v226, v116, v117\n"
    "v_mfma_f32_32x32x16_bf16 v[128:143], a[0:3], v[96:99], v[128:143]\n"
    "ds_read_b128 a[36:39], v240 offset:41632\n"
    "buffer_load_dwordx4 v15, s[28:31], s35 offen offset:3120 lds\n"
    "v_exp_f32 v118, v118\n"
    "v_mfma_f32_32x32x16_bf16 v[128:143], a[4:7], v[100:103], v[128:143]\n"
    "s_add_u32 m0, s48, 124800\n"
    "ds_read_b128 a[40:43], v240 offset:41856\n"
    "v_exp_f32 v119, v119\n"
    "v_exp_f32 v120, v120\n"
    "v_cvt_pk_bf16_f32 v227, v118, v119\n"
    "v_mfma_f32_32x32x16_bf16 v[128:143], a[8:11], v[104:107], v[128:143]\n"
    "s_waitcnt lgkmcnt(2)\n"
    "s_add_u32 s35, s35, 2048\n"
    "ds_read_b128 a[44:47], v240 offset:41888\n"
    "buffer_load_dwordx4 v12, s[28:31], s35 offen offset:0 lds\n"
    "v_exp_f32 v121, v121\n"
    "v_mfma_f32_32x32x16_bf16 v[128:143], a[12:15], v[108:111], v[128:143]\n"
    "ds_read_b128 a[48:51], v240 offset:42112\n"
    "v_cvt_pk_bf16_f32 v228, v120, v121\n"
    "v_exp_f32 v122, v122\n"
    "v_exp_f32 v123, v123\n"
    "v_mfma_f32_32x32x16_bf16 v[16:31], a[16:19], v[224:227], v[16:31]\n"
    "ds_read_b128 a[52:55], v240 offset:42144\n"
    "v_cvt_pk_bf16_f32 v229, v122, v123\n"
    "v_exp_f32 v124, v124\n"
    "v_exp_f32 v125, v125\n"
    "v_mfma_f32_16x16x32_bf16 v[250:253], a[64:67], v[224:227], v[250:253]\n"
    "ds_read_b128 a[56:59], v240 offset:42368\n"
    "v_cvt_pk_bf16_f32 v230, v124, v125\n"
    "v_exp_f32 v126, v126\n"
    "v_exp_f32 v127, v127\n"
    "s_nop 0\n"
    "v_cvt_pk_bf16_f32 v231, v126, v127\n"
    "s_nop 1\n"
    "v_mfma_f32_32x32x16_bf16 v[16:31], a[24:27], v[228:231], v[16:31]\n"
    "ds_read_b128 a[60:63], v240 offset:42400\n"
    "buffer_load_dwordx4 v13, s[28:31], s35 offen offset:1040 lds\n"
    "v_exp_f32 v128, v128\n"
    "v_mfma_f32_16x16x32_bf16 v[250:253], a[64:67], v[228:231], v[250:253]\n"
    "ds_read_b64_tr_b16 a[0:1], v243 offset:49920\n"
    "ds_read_b64_tr_b16 a[2:3], v243 offset:52000\n"
    "v_mfma_f32_32x32x16_bf16 v[112:127], a[32:35], v[80:83], v[144:159]\n"
    "s_waitcnt lgkmcnt(3)\n"
    "v_exp_f32 v129, v129\n"
    "v_exp_f32 v130, v130\n"
    "v_cvt_pk_bf16_f32 v232, v128, v129\n"
    "v_mfma_f32_32x32x16_bf16 v[112:127], a[36:39], v[84:87], v[112:127]\n"
    "ds_read_b64_tr_b16 a[8:9], v243 offset:54080\n"
    "ds_read_b64_tr_b16 a[10:11], v243 offset:56160\n"
    "buffer_load_dwordx4 v14, s[28:31], s35 offen offset:2080 lds\n"
    "v_exp_f32 v131, v131\n"
    "v_mfma_f32_32x32x16_bf16 v[112:127], a[40:43], v[88:91], v[112:127]\n"
    "s_waitcnt vmcnt(15)\n"
    "v_cvt_pk_bf16_f32 v233, v130, v131\n"
    "v_exp_f32 v132, v132\n"
    "v_exp_f32 v133, v133\n"
    "v_mfma_f32_32x32x16_bf16 v[112:127], a[44:47], v[92:95], v[112:127]\n"
    "s_barrier\n"
    "ds_read_b128 a[16:19], v241 offset:0\n"
    "v_cvt_pk_bf16_f32 v234, v132, v133\n"
    "v_mfma_f32_32x32x16_bf16 v[112:127], a[48:51], v[96:99], v[112:127]\n"
    "ds_read_b128 a[20:23], v241 offset:32\n"
    "buffer_load_dwordx4 v15, s[28:31], s35 offen offset:3120 lds\n"
    "v_exp_f32 v134, v134\n"
    "v_mfma_f32_32x32x16_bf16 v[112:127], a[52:55], v[100:103], v[112:127]\n"
    "s_add_u32 m0, s44, 149760\n"
    "ds_read_b128 a[24:27], v241 offset:256\n"
    "v_exp_f32 v135, v135\n"
    "v_exp_f32 v136, v136\n"
    "v_cvt_pk_bf16_f32 v235, v134, v135\n"
    "v_mfma_f32_32x32x16_bf16 v[112:127], a[56:59], v[104:107], v[112:127]\n"
    "s_waitcnt lgkmcnt(2)\n"
    "s_add_u32 s49, s34, 30720\n"
    "ds_read_b128 a[28:31], v241 offset:288\n"
    "buffer_load_dwordx4 v12, s[24:27], s49 offen offset:0 lds\n"
    "v_exp_f32 v137, v137\n"
    "v_mfma_f32_32x32x16_bf16 v[112:127], a[60:63], v[108:111], v[112:127]\n"
    "ds_read_b128 a[32:35], v241 offset:512\n"
    "v_cvt_pk_bf16_f32 v236, v136, v137\n"
    "v_exp_f32 v138, v138\n"
    "v_exp_f32 v139, v139\n"
    "v_mfma_f32_32x32x16_bf16 v[16:31], a[0:3], v[232:235], v[16:31]\n"
    "ds_read_b128 a[36:39], v241 offset:544\n"
    "v_cvt_pk_bf16_f32 v237, v138, v139\n"
    "v_exp_f32 v140, v140\n"
    "v_exp_f32 v141, v141\n"
    "v_mfma_f32_16x16x32_bf16 v[250:253], a[64:67], v[232:235], v[250:253]\n"
    "ds_read_b128 a[40:43], v241 offset:768\n"
    "v_cvt_pk_bf16_f32 v238, v140, v141\n"
    "v_exp_f32 v142, v142\n"
    "v_exp_f32 v143, v143\n"
    "s_nop 0\n"
    "v_cvt_pk_bf16_f32 v239, v142, v143\n"
    "s_nop 1\n"
    "v_mfma_f32_32x32x16_bf16 v[16:31], a[8:11], v[236:239], v[16:31]\n"
    "ds_read_b128 a[44:47], v241 offset:800\n"
    "v_exp_f32 v112, v112\n"
    "v_exp_f32 v113, v113\n"
    "v_mfma_f32_16x16x32_bf16 v[250:253], a[64:67], v[236:239], v[250:253]\n"
    "ds_read_b64_tr_b16 a[48:49], v244 offset:8320\n"
    "ds_read_b64_tr_b16 a[50:51], v244 offset:10400\n"
    "v_mfma_f32_32x32x16_bf16 v[128:143], a[16:19], v[80:83], v[144:159]\n"
    "s_waitcnt lgkmcnt(3)\n"
    "v_cvt_pk_bf16_f32 v160, v112, v113\n"
    "v_exp_f32 v114, v114\n"
    "v_exp_f32 v115, v115\n"
    "v_mfma_f32_32x32x16_bf16 v[128:143], a[20:23], v[84:87], v[128:143]\n"
    "ds_read_b64_tr_b16 a[56:57], v244 offset:12480\n"
    "ds_read_b64_tr_b16 a[58:59], v244 offset:14560\n"
    "v_cvt_pk_bf16_f32 v161, v114, v115\n"
    "v_exp_f32 v116, v116\n"
    "v_mfma_f32_32x32x16_bf16 v[128:143], a[24:27], v[88:91], v[128:143]\n"
    "s_waitcnt vmcnt(13)\n"
    "buffer_load_dwordx4 v13, s[24:27], s49 offen offset:1040 lds\n"
    "v_exp_f32 v117, v117\n"
    "v_exp_f32 v118, v118\n"
    "v_mfma_f32_32x32x16_bf16 v[128:143], a[28:31], v[92:95], v[128:143]\n"
    "s_barrier\n"
    "ds_read_b128 a[0:3], v241 offset:16640\n"
    "v_cvt_pk_bf16_f32 v162, v116, v117\n"
    "v_mfma_f32_32x32x16_bf16 v[128:143], a[32:35], v[96:99], v[128:143]\n"
    "ds_read_b128 a[4:7], v241 offset:16672\n"
    "v_exp_f32 v119, v119\n"
    "v_exp_f32 v120, v120\n"
    "v_mfma_f32_32x32x16_bf16 v[128:143], a[36:39], v[100:103], v[128:143]\n"
    "ds_read_b128 a[8:11], v241 offset:16896\n"
    "v_cvt_pk_bf16_f32 v163, v118, v119\n"
    "v_exp_f32 v121, v121\n"
    "v_exp_f32 v122, v122\n"
    "v_mfma_f32_32x32x16_bf16 v[128:143], a[40:43], v[104:107], v[128:143]\n"
    "s_waitcnt lgkmcnt(2)\n"
    "ds_read_b128 a[12:15], v241 offset:16928\n"
    "v_cvt_pk_bf16_f32 v164, v120, v121\n"
    "v_exp_f32 v123, v123\n"
    "v_exp_f32 v124, v124\n"
    "v_mfma_f32_32x32x16_bf16 v[128:143], a[44:47], v[108:111], v[128:143]\n"
    "ds_read_b128 a[16:19], v241 offset:17152\n"
    "v_cvt_pk_bf16_f32 v165, v122, v123\n"
    "v_exp_f32 v125, v125\n"
    "v_mfma_f32_32x32x16_bf16 v[16:31], a[48:51], v[160:163], v[16:31]\n"
    "ds_read_b128 a[20:23], v241 offset:17184\n"
    "v_cvt_pk_bf16_f32 v166, v124, v125\n"
    "v_exp_f32 v126, v126\n"
    "v_exp_f32 v127, v127\n"
    "v_mfma_f32_16x16x32_bf16 v[250:253], a[64:67], v[160:163], v[250:253]\n"
    "ds_read_b128 a[24:27], v241 offset:17408\n"
    "v_cvt_pk_bf16_f32 v167, v126, v127\n"
    "s_nop 1\n"
    "v_mfma_f32_32x32x16_bf16 v[16:31], a[56:59], v[164:167], v[16:31]\n"
    "ds_read_b128 a[28:31], v241 offset:17440\n"
    "v_exp_f32 v128, v128\n"
    "v_exp_f32 v129, v129\n"
    "v_mfma_f32_16x16x32_bf16 v[250:253], a[64:67], v[164:167], v[250:253]\n"
    "ds_read_b64_tr_b16 a[32:33], v244 offset:24960\n"
    "ds_read_b64_tr_b16 a[34:35], v244 offset:27040\n"
    "v_mfma_f32_32x32x16_bf16 v[112:127], a[0:3], v[80:83], v[144:159]\n"
    "s_waitcnt lgkmcnt(3)\n"
    "v_cvt_pk_bf16_f32 v168, v128, v129\n"
    "v_exp_f32 v130, v130\n"
    "v_exp_f32 v131, v131\n"
    "v_mfma_f32_32x32x16_bf16 v[112:127], a[4:7], v[84:87], v[112:127]\n"
    "ds_read_b64_tr_b16 a[40:41], v244 offset:29120\n"
    "ds_read_b64_tr_b16 a[42:43], v244 offset:31200\n"
    "v_cvt_pk_bf16_f32 v169, v130, v131\n"
    "v_exp_f32 v132, v132\n"
    "v_mfma_f32_32x32x16_bf16 v[112:127], a[8:11], v[88:91], v[112:127]\n"
    "s_waitcnt vmcnt(10)\n"
    "v_exp_f32 v133, v133\n"
    "v_exp_f32 v134, v134\n"
    "v_cvt_pk_bf16_f32 v170, v132, v133\n"
    "v_mfma_f32_32x32x16_bf16 v[112:127], a[12:15], v[92:95], v[112:127]\n"
    "s_barrier\n"
    "ds_read_b128 a[48:51], v241 offset:33280\n"
    "v_mfma_f32_32x32x16_bf16 v[112:127], a[16:19], v[96:99], v[112:127]\n"
    "ds_read_b128 a[52:55], v241 offset:33312\n"
    "v_exp_f32 v135, v135\n"
    "v_exp_f32 v136, v136\n"
    "v_cvt_pk_bf16_f32 v171, v134, v135\n"
    "v_mfma_f32_32x32x16_bf16 v[112:127], a[20:23], v[100:103], v[112:127]\n"
    "ds_read_b128 a[56:59], v241 offset:33536\n"
    "v_exp_f32 v137, v137\n"
    "v_exp_f32 v138, v138\n"
    "v_cvt_pk_bf16_f32 v172, v136, v137\n"
    "v_mfma_f32_32x32x16_bf16 v[112:127], a[24:27], v[104:107], v[112:127]\n"
    "s_waitcnt lgkmcnt(2)\n"
    "ds_read_b128 a[60:63], v241 offset:33568\n"
    "v_exp_f32 v139, v139\n"
    "v_exp_f32 v140, v140\n"
    "v_mfma_f32_32x32x16_bf16 v[112:127], a[28:31], v[108:111], v[112:127]\n"
    ".Lq_tp1_%=:\n"
    "s_cbranch_execz .Lq_tret_%=\n"
    "ds_read_b128 a[0:3], v241 offset:33792\n"
    "v_cvt_pk_bf16_f32 v173, v138, v139\n"
    "v_exp_f32 v141, v141\n"
    "v_mfma_f32_32x32x16_bf16 v[16:31], a[32:35], v[168:171], v[16:31]\n"
    "s_mov_b32 s20, 0x55555555\n"
    "ds_read_b128 a[4:7], v241 offset:33824\n"
    "v_cvt_pk_bf16_f32 v174, v140, v141\n"
    "v_exp_f32 v142, v142\n"
    "v_exp_f32 v143, v143\n"
    "v_mfma_f32_16x16x32_bf16 v[250:253], a[64:67], v[168:171], v[250:253]\n"
    "s_mov_b32 s21, 0x55555555\n"
    "ds_read_b128 a[8:11], v241 offset:34048\n"
    "v_cvt_pk_bf16_f32 v175, v142, v143\n"
    "s_nop 1\n"
    "v_mfma_f32_32x32x16_bf16 v[16:31], a[40:43], v[172:175], v[16:31]\n"
    "s_mov_b32 s16, 0x33333333\n"
    "ds_read_b128 a[12:15], v241 offset:34080\n"
    "v_exp_f32 v112, v112\n"
    "v_exp_f32 v113, v113\n"
    "v_mfma_f32_16x16x32_bf16 v[250:253], a[64:67], v[172:175], v[250:253]\n"
    "s_mov_b32 s17, 0x33333333\n"
    "ds_read_b64_tr_b16 a[16:17], v244 offset:41600\n"
    "ds_read_b64_tr_b16 a[18:19], v244 offset:43680\n"
    "v_mfma_f32_32x32x16_bf16 v[128:143], a[48:51], v[80:83], v[144:159]\n"
    "s_waitcnt lgkmcnt(3)\n"
    "v_cvt_pk_bf16_f32 v176, v112, v113\n"
    "v_exp_f32 v114, v114\n"
    "v_exp_f32 v115, v115\n"
    "v_mfma_f32_32x32x16_bf16 v[128:143], a[52:55], v[84:87], v[128:143]\n"
    "ds_read_b64_tr_b16 a[24:25], v244 offset:45760\n"
    "ds_read_b64_tr_b16 a[26:27], v244 offset:47840\n"
    "v_cvt_pk_bf16_f32 v177, v114, v115\n"
    "v_exp_f32 v116, v116\n"
    "v_mfma_f32_32x32x16_bf16 v[128:143], a[56:59], v[88:91], v[128:143]\n"
    "s_waitcnt vmcnt(6)\n"
    "v_exp_f32 v117, v117\n"
    "v_exp_f32 v118, v118\n"
    "v_cvt_pk_bf16_f32 v178, v116, v117\n"
    "v_mfma_f32_32x32x16_bf16 v[128:143], a[60:63], v[92:95], v[128:143]\n"
    "s_barrier\n"
    "ds_read_b128 a[32:35], v241 offset:49920\n"
    "v_mfma_f32_32x32x16_bf16 v[128:143], a[0:3], v[96:99], v[128:143]\n"
    "ds_read_b128 a[36:39], v241 offset:49952\n"
    "v_exp_f32 v119, v119\n"
    "v_exp_f32 v120, v120\n"
    "v_cvt_pk_bf16_f32 v179, v118, v119\n"
    "v_mfma_f32_32x32x16_bf16 v[128:143], a[4:7], v[100:103], v[128:143]\n"
    "ds_read_b128 a[40:43], v241 offset:50176\n"
    "v_exp_f32 v121, v121\n"
    "v_exp_f32 v122, v122\n"
    "v_cvt_pk_bf16_f32 v180, v120, v121\n"
    "v_mfma_f32_32x32x16_bf16 v[128:143], a[8:11], v[104:107], v[128:143]\n"
    "s_waitcnt lgkmcnt(2)\n"
    "ds_read_b128 a[44:47], v241 offset:50208\n"
    "v_exp_f32 v123, v123\n"
    "v_exp_f32 v124, v124\n"
    "v_mfma_f32_32x32x16_bf16 v[128:143], a[12:15], v[108:111], v[128:143]\n"
    "ds_read_b128 a[48:51], v241 offset:50432\n"
    "v_cvt_pk_bf16_f32 v181, v122, v123\n"
    "v_exp_f32 v125, v125\n"
    "v_mfma_f32_32x32x16_bf16 v[16:31], a[16:19], v[176:179], v[16:31]\n"
    "ds_read_b128 a[52:55], v241 offset:50464\n"
    "v_cvt_pk_bf16_f32 v182, v124, v125\n"
    "v_exp_f32 v126, v126\n"
    "v_exp_f32 v127, v127\n"
    "v_mfma_f32_16x16x32_bf16 v[250:253], a[64:67], v[176:179], v[250:253]\n"
    "ds_read_b128 a[56:59], v241 offset:50688\n"
    "v_cvt_pk_bf16_f32 v183, v126, v127\n"
    "s_nop 1\n"
    "v_mfma_f32_32x32x16_bf16 v[16:31], a[24:27], v[180:183], v[16:31]\n"
    "ds_read_b128 a[60:63], v241 offset:50720\n"
    "v_exp_f32 v128, v128\n"
    "v_exp_f32 v129, v129\n"
    "v_mfma_f32_16x16x32_bf16 v[250:253], a[64:67], v[180:183], v[250:253]\n"
    "ds_read_b64_tr_b16 a[0:1], v245 offset:0\n"
    "ds_read_b64_tr_b16 a[2:3], v245 offset:2080\n"
    "v_mfma_f32_32x32x16_bf16 v[112:127], a[32:35], v[80:83], v[144:159]\n"
    "s_waitcnt lgkmcnt(3)\n"
    "v_cvt_pk_bf16_f32 v184, v128, v129\n"
    "v_exp_f32 v130, v130\n"
    "v_exp_f32 v131, v131\n"
    "v_mfma_f32_32x32x16_bf16 v[112:127], a[36:39], v[84:87], v[112:127]\n"
    "ds_read_b64_tr_b16 a[8:9], v245 offset:4160\n"
    "ds_read_b64_tr_b16 a[10:11], v245 offset:6240\n"
    "v_cvt_pk_bf16_f32 v185, v130, v131\n"
    "v_exp_f32 v132, v132\n"
    "v_mfma_f32_32x32x16_bf16 v[112:127], a[40:43], v[88:91], v[112:127]\n"
    "s_waitcnt vmcnt(2)\n"
    "v_exp_f32 v133, v133\n"
    "v_exp_f32 v134, v134\n"
    "v_cvt_pk_bf16_f32 v186, v132, v133\n"
    "v_mfma_f32_32x32x16_bf16 v[112:127], a[44:47], v[92:95], v[112:127]\n"
    "s_barrier\n"
    "ds_read_b128 a[16:19], v242 offset:8320\n"
    "v_mfma_f32_32x32x16_bf16 v[112:127], a[48:51], v[96:99], v[112:127]\n"
    "ds_read_b128 a[20:23], v242 offset:8352\n"
    "v_exp_f32 v135, v135\n"
    "v_exp_f32 v136, v136\n"
    "v_cvt_pk_bf16_f32 v187, v134, v135\n"
    "v_mfma_f32_32x32x16_bf16 v[112:127], a[52:55], v[100:103], v[112:127]\n"
    "ds_read_b128 a[24:27], v242 offset:8576\n"
    "v_exp_f32 v137, v137\n"
    "v_exp_f32 v138, v138\n"
    "v_cvt_pk_bf16_f32 v188, v136, v137\n"
    "v_mfma_f32_32x32x16_bf16 v[112:127], a[56:59], v[104:107], v[112:127]\n"
    "s_waitcnt lgkmcnt(2)\n"
    "ds_read_b128 a[28:31], v242 offset:8608\n"
    "v_exp_f32 v139, v139\n"
    "v_exp_f32 v140, v140\n"
    "v_mfma_f32_32x32x16_bf16 v[112:127], a[60:63], v[108:111], v[112:127]\n"
    "ds_read_b128 a[32:35], v242 offset:8832\n"
    "v_cvt_pk_bf16_f32 v189, v138, v139\n"
    "v_exp_f32 v141, v141\n"
    "v_mfma_f32_32x32x16_bf16 v[16:31], a[0:3], v[184:187], v[16:31]\n"
    "ds_read_b128 a[36:39], v242 offset:8864\n"
    "v_cvt_pk_bf16_f32 v190, v140, v141\n"
    "v_exp_f32 v142, v142\n"
    "v_exp_f32 v143, v143\n"
    "v_mfma_f32_16x16x32_bf16 v[250:253], a[64:67], v[184:187], v[250:253]\n"
    "ds_read_b128 a[40:43], v242 offset:9088\n"
    "v_cvt_pk_bf16_f32 v191, v142, v143\n"
    "s_nop 1\n"
    "v_mfma_f32_32x32x16_bf16 v[16:31], a[8:11], v[188:191], v[16:31]\n"
    "ds_read_b128 a[44:47], v242 offset:9120\n"
    "v_exp_f32 v112, v112\n"
    "v_exp_f32 v113, v113\n"
    "v_mfma_f32_16x16x32_bf16 v[250:253], a[64:67], v[188:191], v[250:253]\n"
    "ds_read_b64_tr_b16 a[48:49], v245 offset:16640\n"
    "ds_read_b64_tr_b16 a[50:51], v245 offset:18720\n"
    "v_mfma_f32_32x32x16_bf16 v[128:143], a[16:19], v[80:83], v[144:159]\n"
    "s_waitcnt lgkmcnt(3)\n"
    "v_cvt_pk_bf16_f32 v192, v112, v113\n"
    "v_exp_f32 v114, v114\n"
    "v_exp_f32 v115, v115\n"
    "v_mfma_f32_32x32x16_bf16 v[128:143], a[20:23], v[84:87], v[128:143]\n"
    "ds_read_b64_tr_b16 a[56:57], v245 offset:20800\n"
    "ds_read_b64_tr_b16 a[58:59], v245 offset:22880\n"
    "v_cvt_pk_bf16_f32 v193, v114, v115\n"
    "v_exp_f32 v116, v116\n"
    "v_mfma_f32_32x32x16_bf16 v[128:143], a[24:27], v[88:91], v[128:143]\n"
    "v_exp_f32 v117, v117\n"
    "v_exp_f32 v118, v118\n"
    "v_cvt_pk_bf16_f32 v194, v116, v117\n"
    "v_mfma_f32_32x32x16_bf16 v[128:143], a[28:31], v[92:95], v[128:143]\n"
    "ds_read_b64_tr_b16 a[0:1], v243 offset:16896\n"
    "ds_read_b64_tr_b16 a[2:3], v243 offset:18976\n"
    "v_exp_f32 v119, v119\n"
    "v_exp_f32 v120, v120\n"
    "v_mfma_f32_32x32x16_bf16 v[128:143], a[32:35], v[96:99], v[128:143]\n"
    "ds_read_b64_tr_b16 a[4:5], v243 offset:21056\n"
    "ds_read_b64_tr_b16 a[6:7], v243 offset:23136\n"
    "v_cvt_pk_bf16_f32 v195, v118, v119\n"
    "v_exp_f32 v121, v121\n"
    "v_mfma_f32_32x32x16_bf16 v[128:143], a[36:39], v[100:103], v[128:143]\n"
    "ds_read_b64_tr_b16 a[8:9], v243 offset:33536\n"
    "ds_read_b64_tr_b16 a[10:11], v243 offset:35616\n"
    "v_cvt_pk_bf16_f32 v196, v120, v121\n"
    "v_exp_f32 v122, v122\n"
    "v_mfma_f32_32x32x16_bf16 v[128:143], a[40:43], v[104:107], v[128:143]\n"
    "s_waitcnt lgkmcnt(4)\n"
    "v_exp_f32 v123, v123\n"
    "v_exp_f32 v124, v124\n"
    "v_cvt_pk_bf16_f32 v197, v122, v123\n"
    "v_mfma_f32_32x32x16_bf16 v[128:143], a[44:47], v[108:111], v[128:143]\n"
    "s_waitcnt vmcnt(0)\n"
    "v_exp_f32 v125, v125\n"
    "v_exp_f32 v126, v126\n"
    "v_cvt_pk_bf16_f32 v198, v124, v125\n"
    "v_mfma_f32_32x32x16_bf16 v[16:31], a[48:51], v[192:195], v[16:31]\n"
    "s_barrier\n"
    "ds_read_b64_tr_b16 a[16:17], v245 offset:33280\n"
    "ds_read_b64_tr_b16 a[18:19], v245 offset:35360\n"
    "v_mfma_f32_16x16x32_bf16 v[250:253], a[64:67], v[192:195], v[250:253]\n"
    "ds_read_b64_tr_b16 a[20:21], v243 offset:37696\n"
    "ds_read_b64_tr_b16 a[22:23], v243 offset:39776\n"
    "v_exp_f32 v127, v127\n"
    "s_nop 0\n"
    "v_cvt_pk_bf16_f32 v199, v126, v127\n"
    "s_nop 1\n"
    "v_mfma_f32_32x32x16_bf16 v[16:31], a[56:59], v[196:199], v[16:31]\n"
    "ds_read_b64_tr_b16 a[24:25], v243 offset:50176\n"
    "ds_read_b64_tr_b16 a[26:27], v243 offset:52256\n"
    "v_exp_f32 v128, v128\n"
    "v_exp_f32 v129, v129\n"
    "v_exp_f32 v130, v130\n"
    "v_cvt_pk_bf16_f32 v200, v128, v129\n"
    "v_mfma_f32_16x16x32_bf16 v[250:253], a[64:67], v[196:199], v[250:253]\n"
    "ds_read_b64_tr_b16 a[28:29], v245 offset:37440\n"
    "ds_read_b64_tr_b16 a[30:31], v245 offset:39520\n"
    "v_exp_f32 v131, v131\n"
    "v_mfma_f32_32x32x16_bf16 v[32:47], a[0:3], v[216:219], v[32:47]\n"
    "s_waitcnt lgkmcnt(4)\n"
    "ds_read_b64_tr_b16 a[32:33], v243 offset:54336\n"
    "ds_read_b64_tr_b16 a[34:35], v243 offset:56416\n"
    "v_cvt_pk_bf16_f32 v201, v130, v131\n"
    "v_exp_f32 v132, v132\n"
    "v_exp_f32 v133, v133\n"
    "v_exp_f32 v134, v134\n"
    "v_mfma_f32_32x32x16_bf16 v[32:47], a[4:7], v[220:223], v[32:47]\n"
    "ds_read_b64_tr_b16 a[36:37], v244 offset:8576\n"
    "ds_read_b64_tr_b16 a[38:39], v244 offset:10656\n"
    "v_cvt_pk_bf16_f32 v202, v132, v133\n"
    "v_exp_f32 v135, v135\n"
    "v_exp_f32 v136, v136\n"
    "v_cvt_pk_bf16_f32 v203, v134, v135\n"
    "v_add_f32 v10, v10, v136\n"
    "v_mfma_f32_32x32x16_bf16 v[32:47], a[8:11], v[224:227], v[32:47]\n"
    "ds_read_b64_tr_b16 a[40:41], v244 offset:12736\n"
    "ds_read_b64_tr_b16 a[42:43], v244 offset:14816\n"
    "v_exp_f32 v137, v137\n"
    "v_exp_f32 v138, v138\n"
    "v_cvt_pk_bf16_f32 v204, v136, v137\n"
    "v_exp_f32 v139, v139\n"
    "v_mfma_f32_16x16x32_bf16 v[250:253], a[64:67], v[200:203], v[250:253]\n"
    "ds_read_b64_tr_b16 a[44:45], v244 offset:25216\n"
    "ds_read_b64_tr_b16 a[46:47], v244 offset:27296\n"
    "v_cvt_pk_bf16_f32 v205, v138, v139\n"
    "v_exp_f32 v140, v140\n"
    "v_mfma_f32_32x32x16_bf16 v[16:31], a[16:19], v[200:203], v[16:31]\n"
    "ds_read_b64_tr_b16 a[48:49], v244 offset:29376\n"
    "ds_read_b64_tr_b16 a[50:51], v244 offset:31456\n"
    "v_exp_f32 v141, v141\n"
    "v_exp_f32 v142, v142\n"
    "v_cvt_pk_bf16_f32 v206, v140, v141\n"
    "v_exp_f32 v143, v143\n"
    "v_mfma_f32_32x32x16_bf16 v[32:47], a[20:23], v[228:231], v[32:47]\n"
    "s_waitcnt lgkmcnt(4)\n"
    "ds_read_b64_tr_b16 a[52:53], v244 offset:41856\n"
    "ds_read_b64_tr_b16 a[54:55], v244 offset:43936\n"
    "v_cvt_pk_bf16_f32 v207, v142, v143\n"
    "v_add_f32 v11, v11, v137\n"
    "v_add_f32 v10, v10, v138\n"
    "v_add_f32 v11, v11, v139\n"
    "v_add_f32 v10, v10, v140\n"
    "v_add_f32 v11, v11, v141\n"
    "v_add_f32 v10, v10, v142\n"
    "v_mfma_f32_32x32x16_bf16 v[32:47], a[24:27], v[232:235], v[32:47]\n"
    "ds_read_b64_tr_b16 a[56:57], v244 offset:46016\n"
    "ds_read_b64_tr_b16 a[58:59], v244 offset:48096\n"
    "v_add_f32 v11, v11, v143\n"
    "v_add_f32 v10, v10, v11\n"
    "v_mov_b32 v11, v10\n"
    "s_nop 1\n"
    "v_permlane32_swap_b32 v10, v11\n"
    "v_mfma_f32_32x32x16_bf16 v[16:31], a[28:31], v[204:207], v[16:31]\n"
    "ds_read_b64_tr_b16 a[60:61], v245 offset:256\n"
    "ds_read_b64_tr_b16 a[62:63], v245 offset:2336\n"
    "v_add_f32 v10, v10, v11\n"
    "v_add_f32 v10, v10, v250\n"
    "v_rcp_f32 v6, v10\n"
    "v_cmp_nge_f32 vcc, s51, v10\n"
    "v_mov_b32 v7, v6\n"
    "v_mfma_f32_32x32x16_bf16 v[32:47], a[32:35], v[236:239], v[32:47]\n"
    "ds_read_b64_tr_b16 a[0:1], v245 offset:4416\n"
    "ds_read_b64_tr_b16 a[2:3], v245 offset:6496\n"
    "s_cbranch_vccnz .Lq_fb_%=\n"
    "v_mfma_f32_32x32x16_bf16 v[32:47], a[36:39], v[160:163], v[32:47]\n"
    "ds_read_b64_tr_b16 a[4:5], v245 offset:16896\n"
    "ds_read_b64_tr_b16 a[6:7], v245 offset:18976\n"
    "v_mul_f32 v16, v6, v16\n"
    "v_mul_f32 v17, v6, v17\n"
    "v_mul_f32 v18, v6, v18\n"
    "v_mul_f32 v19, v6, v19\n"
    "v_mul_f32 v20, v6, v20\n"
    "v_mul_f32 v21, v6, v21\n"
    "v_mfma_f32_32x32x16_bf16 v[32:47], a[40:43], v[164:167], v[32:47]\n"
    "s_waitcnt lgkmcnt(4)\n"
    "ds_read_b64_tr_b16 a[8:9], v245 offset:21056\n"
    "ds_read_b64_tr_b16 a[10:11], v245 offset:23136\n"
    "v_mul_f32 v22, v6, v22\n"
    "v_mul_f32 v23, v6, v23\n"
    "v_mul_f32 v24, v6, v24\n"
    "v_mul_f32 v25, v6, v25\n"
    "v_mul_f32 v26, v6, v26\n"
    "v_mul_f32 v27, v6, v27\n"
    "v_mfma_f32_32x32x16_bf16 v[32:47], a[44:47], v[168:171], v[32:47]\n"
    "ds_read_b64_tr_b16 a[12:13], v245 offset:33536\n"
    "ds_read_b64_tr_b16 a[14:15], v245 offset:35616\n"
    "v_mul_f32 v28, v6, v28\n"
    "v_mul_f32 v29, v6, v29\n"
    "v_mul_f32 v30, v6, v30\n"
    "v_mul_f32 v31, v6, v31\n"
    "v_cvt_pk_bf16_f32 v144, v16, v17\n"
    "v_cvt_pk_bf16_f32 v145, v18, v19\n"
    "v_cvt_pk_bf16_f32 v146, v20, v21\n"
    "v_mfma_f32_32x32x16_bf16 v[32:47], a[48:51], v[172:175], v[32:47]\n"
    "ds_read_b64_tr_b16 a[16:17], v245 offset:37696\n"
    "ds_read_b64_tr_b16 a[18:19], v245 offset:39776\n"
    "v_cvt_pk_bf16_f32 v147, v22, v23\n"
    "v_cvt_pk_bf16_f32 v148, v24, v25\n"
    "v_cvt_pk_bf16_f32 v149, v26, v27\n"
    "v_cvt_pk_bf16_f32 v150, v28, v29\n"
    "v_cvt_pk_bf16_f32 v151, v30, v31\n"
    "s_mov_b64 vcc, s[20:21]\n"
    "v_cndmask_b32_dpp v112, v146, v144, vcc quad_perm:[1,0,3,2] row_mask:0xf bank_mask:0xf\n"
    "v_mfma_f32_32x32x16_bf16 v[32:47], a[52:55], v[176:179], v[32:47]\n"
    "ds_read_b64_tr_b16 a[20:21], v243 offset:17152\n"
    "ds_read_b64_tr_b16 a[22:23], v243 offset:19232\n"
    "v_cndmask_b32_dpp v113, v147, v145, vcc quad_perm:[1,0,3,2] row_mask:0xf bank_mask:0xf\n"
    "v_cndmask_b32_dpp v116, v150, v148, vcc quad_perm:[1,0,3,2] row_mask:0xf bank_mask:0xf\n"
    "v_cndmask_b32_dpp v117, v151, v149, vcc quad_perm:[1,0,3,2] row_mask:0xf bank_mask:0xf\n"
    "s_not_b64 vcc, s[20:21]\n"
    "v_cndmask_b32_dpp v114, v144, v146, vcc quad_perm:[1,0,3,2] row_mask:0xf bank_mask:0xf\n"
    "v_cndmask_b32_dpp v115, v145, v147, vcc quad_perm:[1,0,3,2] row_mask:0xf bank_mask:0xf\n"
    "v_cndmask_b32_dpp v118, v148, v150, vcc quad_perm:[1,0,3,2] row_mask:0xf bank_mask:0xf\n"
    "v_mfma_f32_32x32x16_bf16 v[32:47], a[56:59], v[180:183], v[32:47]\n"
    "ds_read_b64_tr_b16 a[24:25], v243 offset:21312\n"
    "ds_read_b64_tr_b16 a[26:27], v243 offset:23392\n"
    "v_cndmask_b32_dpp v119, v149, v151, vcc quad_perm:[1,0,3,2] row_mask:0xf bank_mask:0xf\n"
    "s_mov_b64 vcc, s[16:17]\n"
    "v_cndmask_b32_dpp v80, v116, v112, vcc quad_perm:[2,3,0,1] row_mask:0xf bank_mask:0xf\n"
    "v_cndmask_b32_dpp v81, v117, v113, vcc quad_perm:[2,3,0,1] row_mask:0xf bank_mask:0xf\n"
    "v_cndmask_b32_dpp v84, v118, v114, vcc quad_perm:[2,3,0,1] row_mask:0xf bank_mask:0xf\n"
    "v_cndmask_b32_dpp v85, v119, v115, vcc quad_perm:[2,3,0,1] row_mask:0xf bank_mask:0xf\n"
    "s_not_b64 vcc, s[16:17]\n"
    "v_mfma_f32_32x32x16_bf16 v[32:47], a[60:63], v[184:187], v[32:47]\n"
    "s_waitcnt lgkmcnt(4)\n"
    "ds_read_b64_tr_b16 a[28:29], v243 offset:33792\n"
    "ds_read_b64_tr_b16 a[30:31], v243 offset:35872\n"
    "v_cndmask_b32_dpp v82, v112, v116, vcc quad_perm:[2,3,0,1] row_mask:0xf bank_mask:0xf\n"
    "v_cndmask_b32_dpp v83, v113, v117, vcc quad_perm:[2,3,0,1] row_mask:0xf bank_mask:0xf\n"
    "v_cndmask_b32_dpp v86, v114, v118, vcc quad_perm:[2,3,0,1] row_mask:0xf bank_mask:0xf\n"
    "v_cndmask_b32_dpp v87, v115, v119, vcc quad_perm:[2,3,0,1] row_mask:0xf bank_mask:0xf\n"
    "v_permlane32_swap_b32 v80, v82\n"
    "v_mfma_f32_32x32x16_bf16 v[32:47], a[0:3], v[188:191], v[32:47]\n"
    "ds_read_b64_tr_b16 a[32:33], v243 offset:37952\n"
    "ds_read_b64_tr_b16 a[34:35], v243 offset:40032\n"
    "v_permlane32_swap_b32 v81, v83\n"
    "v_permlane32_swap_b32 v84, v86\n"
    "v_permlane32_swap_b32 v85, v87\n"
    "v_mfma_f32_32x32x16_bf16 v[32:47], a[4:7], v[192:195], v[32:47]\n"
    "ds_read_b64_tr_b16 a[36:37], v243 offset:50432\n"
    "ds_read_b64_tr_b16 a[38:39], v243 offset:52512\n"
    "buffer_store_dwordx4 v[80:83], v246, s[36:39], 0 offen offset:0 sc1 nt\n"
    "v_mfma_f32_32x32x16_bf16 v[32:47], a[8:11], v[196:199], v[32:47]\n"
    "ds_read_b64_tr_b16 a[40:41], v243 offset:54592\n"
    "ds_read_b64_tr_b16 a[42:43], v243 offset:56672\n"
    "v_mfma_f32_32x32x16_bf16 v[32:47], a[12:15], v[200:203], v[32:47]\n"
    "ds_read_b64_tr_b16 a[44:45], v244 offset:8832\n"
    "ds_read_b64_tr_b16 a[46:47], v244 offset:10912\n"
    "v_mfma_f32_32x32x16_bf16 v[32:47], a[16:19], v[204:207], v[32:47]\n"
    "s_waitcnt lgkmcnt(4)\n"
    "ds_read_b64_tr_b16 a[48:49], v244 offset:12992\n"
    "ds_read_b64_tr_b16 a[50:51], v244 offset:15072\n"
    "v_mfma_f32_32x32x16_bf16 v[48:63], a[20:23], v[216:219], v[48:63]\n"
    "ds_read_b64_tr_b16 a[52:53], v244 offset:25472\n"
    "ds_read_b64_tr_b16 a[54:55], v244 offset:27552\n"
    "buffer_store_dwordx4 v[84:87], v246, s[36:39], 0 offen offset:256 sc1 nt\n"
    "v_mfma_f32_32x32x16_bf16 v[48:63], a[24:27], v[220:223], v[48:63]\n"
    "ds_read_b64_tr_b16 a[56:57], v244 offset:29632\n"
    "ds_read_b64_tr_b16 a[58:59], v244 offset:31712\n"
    "v_mfma_f32_32x32x16_bf16 v[48:63], a[28:31], v[224:227], v[48:63]\n"
    "ds_read_b64_tr_b16 a[60:61], v244 offset:42112\n"
    "ds_read_b64_tr_b16 a[62:63], v244 offset:44192\n"
    "v_mul_f32 v32, v6, v32\n"
    "v_mul_f32 v33, v6, v33\n"
    "v_mul_f32 v34, v6, v34\n"
    "v_mul_f32 v35, v6, v35\n"
    "v_mfma_f32_32x32x16_bf16 v[48:63], a[32:35], v[228:231], v[48:63]\n"
    "ds_read_b64_tr_b16 a[0:1], v244 offset:46272\n"
    "ds_read_b64_tr_b16 a[2:3], v244 offset:48352\n"
    "v_mul_f32 v36, v6, v36\n"
    "v_mul_f32 v37, v6, v37\n"
    "v_mul_f32 v38, v6, v38\n"
    "v_mul_f32 v39, v6, v39\n"
    "v_mfma_f32_32x32x16_bf16 v[48:63], a[36:39], v[232:235], v[48:63]\n"
    "s_waitcnt lgkmcnt(4)\n"
    "ds_read_b64_tr_b16 a[4:5], v245 offset:512\n"
    "ds_read_b64_tr_b16 a[6:7], v245 offset:2592\n"
    "v_mul_f32 v40, v6, v40\n"
    "v_mul_f32 v41, v6, v41\n"
    "v_mul_f32 v42, v6, v42\n"
    "v_mul_f32 v43, v6, v43\n"
    "v_mfma_f32_32x32x16_bf16 v[48:63], a[40:43], v[236:239], v[48:63]\n"
    "ds_read_b64_tr_b16 a[8:9], v245 offset:4672\n"
    "ds_read_b64_tr_b16 a[10:11], v245 offset:6752\n"
    "v_mul_f32 v44, v6, v44\n"
    "v_mul_f32 v45, v6, v45\n"
    "v_mul_f32 v46, v6, v46\n"
    "v_mul_f32 v47, v6, v47\n"
    "v_mfma_f32_32x32x16_bf16 v[48:63], a[44:47], v[160:163], v[48:63]\n"
    "ds_read_b64_tr_b16 a[12:13], v245 offset:17152\n"
    "ds_read_b64_tr_b16 a[14:15], v245 offset:19232\n"
    "v_cvt_pk_bf16_f32 v152, v32, v33\n"
    "v_cvt_pk_bf16_f32 v153, v34, v35\n"
    "v_cvt_pk_bf16_f32 v154, v36, v37\n"
    "v_cvt_pk_bf16_f32 v155, v38, v39\n"
    "v_mfma_f32_32x32x16_bf16 v[48:63], a[48:51], v[164:167], v[48:63]\n"
    "ds_read_b64_tr_b16 a[16:17], v245 offset:21312\n"
    "ds_read_b64_tr_b16 a[18:19], v245 offset:23392\n"
    "v_cvt_pk_bf16_f32 v156, v40, v41\n"
    "v_cvt_pk_bf16_f32 v157, v42, v43\n"
    "v_cvt_pk_bf16_f32 v158, v44, v45\n"
    "v_cvt_pk_bf16_f32 v159, v46, v47\n"
    "v_mfma_f32_32x32x16_bf16 v[48:63], a[52:55], v[168:171], v[48:63]\n"
    "s_mov_b64 vcc, s[20:21]\n"
    "ds_read_b64_tr_b16 a[20:21], v245 offset:33792\n"
    "ds_read_b64_tr_b16 a[22:23], v245 offset:35872\n"
    "v_cndmask_b32_dpp v120, v154, v152, vcc quad_perm:[1,0,3,2] row_mask:0xf bank_mask:0xf\n"
    "v_cndmask_b32_dpp v121, v155, v153, vcc quad_perm:[1,0,3,2] row_mask:0xf bank_mask:0xf\n"
    "v_cndmask_b32_dpp v124, v158, v156, vcc quad_perm:[1,0,3,2] row_mask:0xf bank_mask:0xf\n"
    "v_cndmask_b32_dpp v125, v159, v157, vcc quad_perm:[1,0,3,2] row_mask:0xf bank_mask:0xf\n"
    "v_mfma_f32_32x32x16_bf16 v[48:63], a[56:59], v[172:175], v[48:63]\n"
    "s_waitcnt lgkmcnt(4)\n"
    "ds_read_b64_tr_b16 a[24:25], v245 offset:37952\n"
    "ds_read_b64_tr_b16 a[26:27], v245 offset:40032\n"
    "s_not_b64 vcc, s[20:21]\n"
    "v_cndmask_b32_dpp v122, v152, v154, vcc quad_perm:[1,0,3,2] row_mask:0xf bank_mask:0xf\n"
    "v_cndmask_b32_dpp v123, v153, v155, vcc quad_perm:[1,0,3,2] row_mask:0xf bank_mask:0xf\n"
    "v_cndmask_b32_dpp v126, v156, v158, vcc quad_perm:[1,0,3,2] row_mask:0xf bank_mask:0xf\n"
    "v_mfma_f32_32x32x16_bf16 v[48:63], a[60:63], v[176:179], v[48:63]\n"
    "ds_read_b64_tr_b16 a[28:29], v243 offset:17408\n"
    "ds_read_b64_tr_b16 a[30:31], v243 offset:19488\n"
    "v_cndmask_b32_dpp v127, v157, v159, vcc quad_perm:[1,0,3,2] row_mask:0xf bank_mask:0xf\n"
    "s_mov_b64 vcc, s[16:17]\n"
    "v_cndmask_b32_dpp v88, v124, v120, vcc quad_perm:[2,3,0,1] row_mask:0xf bank_mask:0xf\n"
    "v_cndmask_b32_dpp v89, v125, v121, vcc quad_perm:[2,3,0,1] row_mask:0xf bank_mask:0xf\n"
    "v_mfma_f32_32x32x16_bf16 v[48:63], a[0:3], v[180:183], v[48:63]\n"
    "ds_read_b64_tr_b16 a[32:33], v243 offset:21568\n"
    "ds_read_b64_tr_b16 a[34:35], v243 offset:23648\n"
    "v_cndmask_b32_dpp v92, v126, v122, vcc quad_perm:[2,3,0,1] row_mask:0xf bank_mask:0xf\n"
    "v_cndmask_b32_dpp v93, v127, v123, vcc quad_perm:[2,3,0,1] row_mask:0xf bank_mask:0xf\n"
    "s_not_b64 vcc, s[16:17]\n"
    "v_cndmask_b32_dpp v90, v120, v124, vcc quad_perm:[2,3,0,1] row_mask:0xf bank_mask:0xf\n"
    "v_mfma_f32_32x32x16_bf16 v[48:63], a[4:7], v[184:187], v[48:63]\n"
    "ds_read_b64_tr_b16 a[36:37], v243 offset:34048\n"
    "ds_read_b64_tr_b16 a[38:39], v243 offset:36128\n"
    "v_cndmask_b32_dpp v91, v121, v125, vcc quad_perm:[2,3,0,1] row_mask:0xf bank_mask:0xf\n"
    "v_cndmask_b32_dpp v94, v122, v126, vcc quad_perm:[2,3,0,1] row_mask:0xf bank_mask:0xf\n"
    "v_cndmask_b32_dpp v95, v123, v127, vcc quad_perm:[2,3,0,1] row_mask:0xf bank_mask:0xf\n"
    "v_mfma_f32_32x32x16_bf16 v[48:63], a[8:11], v[188:191], v[48:63]\n"
    "ds_read_b64_tr_b16 a[40:41], v243 offset:38208\n"
    "ds_read_b64_tr_b16 a[42:43], v243 offset:40288\n"
    "v_permlane32_swap_b32 v88, v90\n"
    "v_permlane32_swap_b32 v89, v91\n"
    "v_mfma_f32_32x32x16_bf16 v[48:63], a[12:15], v[192:195], v[48:63]\n"
    "s_waitcnt lgkmcnt(4)\n"
    "ds_read_b64_tr_b16 a[44:45], v243 offset:50688\n"
    "ds_read_b64_tr_b16 a[46:47], v243 offset:52768\n"
    "buffer_store_dwordx4 v[88:91], v246, s[36:39], 0 offen offset:64 sc1 nt\n"
    "v_permlane32_swap_b32 v92, v94\n"
    "v_mfma_f32_32x32x16_bf16 v[48:63], a[16:19], v[196:199], v[48:63]\n"
    "ds_read_b64_tr_b16 a[48:49], v243 offset:54848\n"
    "ds_read_b64_tr_b16 a[50:51], v243 offset:56928\n"
    "v_permlane32_swap_b32 v93, v95\n"
    "v_mfma_f32_32x32x16_bf16 v[48:63], a[20:23], v[200:203], v[48:63]\n"
    "ds_read_b64_tr_b16 a[52:53], v244 offset:9088\n"
    "ds_read_b64_tr_b16 a[54:55], v244 offset:11168\n"
    "v_mfma_f32_32x32x16_bf16 v[48:63], a[24:27], v[204:207], v[48:63]\n"
    "ds_read_b64_tr_b16 a[56:57], v244 offset:13248\n"
    "ds_read_b64_tr_b16 a[58:59], v244 offset:15328\n"
    "v_mfma_f32_32x32x16_bf16 v[64:79], a[28:31], v[216:219], v[64:79]\n"
    "ds_read_b64_tr_b16 a[60:61], v244 offset:25728\n"
    "ds_read_b64_tr_b16 a[62:63], v244 offset:27808\n"
    "buffer_store_dwordx4 v[92:95], v246, s[36:39], 0 offen offset:320 sc1 nt\n"
    "v_mfma_f32_32x32x16_bf16 v[64:79], a[32:35], v[220:223], v[64:79]\n"
    "s_waitcnt lgkmcnt(4)\n"
    "ds_read_b64_tr_b16 a[0:1], v244 offset:29888\n"
    "ds_read_b64_tr_b16 a[2:3], v244 offset:31968\n"
    "v_mfma_f32_32x32x16_bf16 v[64:79], a[36:39], v[224:227], v[64:79]\n"
    "ds_read_b64_tr_b16 a[4:5], v244 offset:42368\n"
    "ds_read_b64_tr_b16 a[6:7], v244 offset:44448\n"
    "v_mul_f32 v48, v6, v48\n"
    "v_mul_f32 v49, v6, v49\n"
    "v_mul_f32 v50, v6, v50\n"
    "v_mul_f32 v51, v6, v51\n"
    "v_mfma_f32_32x32x16_bf16 v[64:79], a[40:43], v[228:231], v[64:79]\n"
    "ds_read_b64_tr_b16 a[8:9], v244 offset:46528\n"
    "ds_read_b64_tr_b16 a[10:11], v244 offset:48608\n"
    "v_mul_f32 v52, v6, v52\n"
    "v_mul_f32 v53, v6, v53\n"
    "v_mul_f32 v54, v6, v54\n"
    "v_mul_f32 v55, v6, v55\n"
    "v_mfma_f32_32x32x16_bf16 v[64:79], a[44:47], v[232:235], v[64:79]\n"
    "ds_read_b64_tr_b16 a[12:13], v245 offset:768\n"
    "ds_read_b64_tr_b16 a[14:15], v245 offset:2848\n"
    "v_mul_f32 v56, v6, v56\n"
    "v_mul_f32 v57, v6, v57\n"
    "v_mul_f32 v58, v6, v58\n"
    "v_mul_f32 v59, v6, v59\n"
    "v_mfma_f32_32x32x16_bf16 v[64:79], a[48:51], v[236:239], v[64:79]\n"
    "ds_read_b64_tr_b16 a[16:17], v245 offset:4928\n"
    "ds_read_b64_tr_b16 a[18:19], v245 offset:7008\n"
    "v_mul_f32 v60, v6, v60\n"
    "v_mul_f32 v61, v6, v61\n"
    "v_mul_f32 v62, v6, v62\n"
    "v_mul_f32 v63, v6, v63\n"
    "v_mfma_f32_32x32x16_bf16 v[64:79], a[52:55], v[160:163], v[64:79]\n"
    "s_waitcnt lgkmcnt(4)\n"
    "ds_read_b64_tr_b16 a[20:21], v245 offset:17408\n"
    "ds_read_b64_tr_b16 a[22:23], v245 offset:19488\n"
    "v_cvt_pk_bf16_f32 v128, v48, v49\n"
    "v_cvt_pk_bf16_f32 v129, v50, v51\n"
    "v_cvt_pk_bf16_f32 v130, v52, v53\n"
    "v_cvt_pk_bf16_f32 v131, v54, v55\n"
    "v_mfma_f32_32x32x16_bf16 v[64:79], a[56:59], v[164:167], v[64:79]\n"
    "ds_read_b64_tr_b16 a[24:25], v245 offset:21568\n"
    "ds_read_b64_tr_b16 a[26:27], v245 offset:23648\n"
    "v_cvt_pk_bf16_f32 v132, v56, v57\n"
    "v_cvt_pk_bf16_f32 v133, v58, v59\n"
    "v_cvt_pk_bf16_f32 v134, v60, v61\n"
    "v_cvt_pk_bf16_f32 v135, v62, v63\n"
    "v_mfma_f32_32x32x16_bf16 v[64:79], a[60:63], v[168:171], v[64:79]\n"
    "s_mov_b64 vcc, s[20:21]\n"
    "ds_read_b64_tr_b16 a[28:29], v245 offset:34048\n"
    "ds_read_b64_tr_b16 a[30:31], v245 offset:36128\n"
    "v_cndmask_b32_dpp v112, v130, v128, vcc quad_perm:[1,0,3,2] row_mask:0xf bank_mask:0xf\n"
    "v_cndmask_b32_dpp v113, v131, v129, vcc quad_perm:[1,0,3,2] row_mask:0xf bank_mask:0xf\n"
    "v_cndmask_b32_dpp v116, v134, v132, vcc quad_perm:[1,0,3,2] row_mask:0xf bank_mask:0xf\n"
    "v_cndmask_b32_dpp v117, v135, v133, vcc quad_perm:[1,0,3,2] row_mask:0xf bank_mask:0xf\n"
    "v_mfma_f32_32x32x16_bf16 v[64:79], a[0:3], v[172:175], v[64:79]\n"
    "s_not_b64 vcc, s[20:21]\n"
    "ds_read_b64_tr_b16 a[32:33], v245 offset:38208\n"
    "ds_read_b64_tr_b16 a[34:35], v245 offset:40288\n"
    "v_cndmask_b32_dpp v114, v128, v130, vcc quad_perm:[1,0,3,2] row_mask:0xf bank_mask:0xf\n"
    "v_cndmask_b32_dpp v115, v129, v131, vcc quad_perm:[1,0,3,2] row_mask:0xf bank_mask:0xf\n"
    "v_cndmask_b32_dpp v118, v132, v134, vcc quad_perm:[1,0,3,2] row_mask:0xf bank_mask:0xf\n"
    "v_cndmask_b32_dpp v119, v133, v135, vcc quad_perm:[1,0,3,2] row_mask:0xf bank_mask:0xf\n"
    "v_mfma_f32_32x32x16_bf16 v[64:79], a[4:7], v[176:179], v[64:79]\n"
    "s_mov_b64 vcc, s[16:17]\n"
    "v_cndmask_b32_dpp v96, v116, v112, vcc quad_perm:[2,3,0,1] row_mask:0xf bank_mask:0xf\n"
    "v_cndmask_b32_dpp v97, v117, v113, vcc quad_perm:[2,3,0,1] row_mask:0xf bank_mask:0xf\n"
    "v_cndmask_b32_dpp v100, v118, v114, vcc quad_perm:[2,3,0,1] row_mask:0xf bank_mask:0xf\n"
    "v_cndmask_b32_dpp v101, v119, v115, vcc quad_perm:[2,3,0,1] row_mask:0xf bank_mask:0xf\n"
    "s_not_b64 vcc, s[16:17]\n"
    "v_cndmask_b32_dpp v98, v112, v116, vcc quad_perm:[2,3,0,1] row_mask:0xf bank_mask:0xf\n"
    "v_mfma_f32_32x32x16_bf16 v[64:79], a[8:11], v[180:183], v[64:79]\n"
    "s_waitcnt lgkmcnt(2)\n"
    "v_cndmask_b32_dpp v99, v113, v117, vcc quad_perm:[2,3,0,1] row_mask:0xf bank_mask:0xf\n"
    "v_cndmask_b32_dpp v102, v114, v118, vcc quad_perm:[2,3,0,1] row_mask:0xf bank_mask:0xf\n"
    "v_cndmask_b32_dpp v103, v115, v119, vcc quad_perm:[2,3,0,1] row_mask:0xf bank_mask:0xf\n"
    "v_permlane32_swap_b32 v96, v98\n"
    "v_mfma_f32_32x32x16_bf16 v[64:79], a[12:15], v[184:187], v[64:79]\n"
    "v_permlane32_swap_b32 v97, v99\n"
    "v_permlane32_swap_b32 v100, v102\n"
    "v_permlane32_swap_b32 v101, v103\n"
    "v_mfma_f32_32x32x16_bf16 v[64:79], a[16:19], v[188:191], v[64:79]\n"
    "buffer_store_dwordx4 v[96:99], v246, s[36:39], 0 offen offset:128 sc1 nt\n"
    "v_mfma_f32_32x32x16_bf16 v[64:79], a[20:23], v[192:195], v[64:79]\n"
    "v_mfma_f32_32x32x16_bf16 v[64:79], a[24:27], v[196:199], v[64:79]\n"
    "v_mfma_f32_32x32x16_bf16 v[64:79], a[28:31], v[200:203], v[64:79]\n"
    "s_waitcnt lgkmcnt(0)\n"
    "v_mfma_f32_32x32x16_bf16 v[64:79], a[32:35], v[204:207], v[64:79]\n"
    "buffer_store_dwordx4 v[100:103], v246, s[36:39], 0 offen offset:384 sc1 nt\n"
    "s_nop 10\n"
    "v_pk_mul_f32 v[64:65], v[64:65], v[6:7]\n"
    "v_pk_mul_f32 v[66:67], v[66:67], v[6:7]\n"
    "v_pk_mul_f32 v[68:69], v[68:69], v[6:7]\n"
    "v_pk_mul_f32 v[70:71], v[70:71], v[6:7]\n"
    "v_pk_mul_f32 v[72:73], v[72:73], v[6:7]\n"
    "v_pk_mul_f32 v[74:75], v[74:75], v[6:7]\n"
    "v_pk_mul_f32 v[76:77], v[76:77], v[6:7]\n"
    "v_pk_mul_f32 v[78:79], v[78:79], v[6:7]\n"
    "v_cvt_pk_bf16_f32 v136, v64, v65\n"
    "v_cvt_pk_bf16_f32 v137, v66, v67\n"
    "v_cvt_pk_bf16_f32 v138, v68, v69\n"
    "v_cvt_pk_bf16_f32 v139, v70, v71\n"
    "v_cvt_pk_bf16_f32 v140, v72, v73\n"
    "v_cvt_pk_bf16_f32 v141, v74, v75\n"
    "v_cvt_pk_bf16_f32 v142, v76, v77\n"
    "v_cvt_pk_bf16_f32 v143, v78, v79\n"
    "s_mov_b64 vcc, s[20:21]\n"
    "v_cndmask_b32_dpp v120, v138, v136, vcc quad_perm:[1,0,3,2] row_mask:0xf bank_mask:0xf\n"
    "v_cndmask_b32_dpp v121, v139, v137, vcc quad_perm:[1,0,3,2] row_mask:0xf bank_mask:0xf\n"
    "v_cndmask_b32_dpp v124, v142, v140, vcc quad_perm:[1,0,3,2] row_mask:0xf bank_mask:0xf\n"
    "v_cndmask_b32_dpp v125, v143, v141, vcc quad_perm:[1,0,3,2] row_mask:0xf bank_mask:0xf\n"
    "s_not_b64 vcc, s[20:21]\n"
    "v_cndmask_b32_dpp v122, v136, v138, vcc quad_perm:[1,0,3,2] row_mask:0xf bank_mask:0xf\n"
    "v_cndmask_b32_dpp v123, v137, v139, vcc quad_perm:[1,0,3,2] row_mask:0xf bank_mask:0xf\n"
    "v_cndmask_b32_dpp v126, v140, v142, vcc quad_perm:[1,0,3,2] row_mask:0xf bank_mask:0xf\n"
    "v_cndmask_b32_dpp v127, v141, v143, vcc quad_perm:[1,0,3,2] row_mask:0xf bank_mask:0xf\n"
    "s_mov_b64 vcc, s[16:17]\n"
    "v_cndmask_b32_dpp v104, v124, v120, vcc quad_perm:[2,3,0,1] row_mask:0xf bank_mask:0xf\n"
    "v_cndmask_b32_dpp v105, v125, v121, vcc quad_perm:[2,3,0,1] row_mask:0xf bank_mask:0xf\n"
    "v_cndmask_b32_dpp v108, v126, v122, vcc quad_perm:[2,3,0,1] row_mask:0xf bank_mask:0xf\n"
    "v_cndmask_b32_dpp v109, v127, v123, vcc quad_perm:[2,3,0,1] row_mask:0xf bank_mask:0xf\n"
    "s_not_b64 vcc, s[16:17]\n"
    "v_cndmask_b32_dpp v106, v120, v124, vcc quad_perm:[2,3,0,1] row_mask:0xf bank_mask:0xf\n"
    "v_cndmask_b32_dpp v107, v121, v125, vcc quad_perm:[2,3,0,1] row_mask:0xf bank_mask:0xf\n"
    "v_cndmask_b32_dpp v110, v122, v126, vcc quad_perm:[2,3,0,1] row_mask:0xf bank_mask:0xf\n"
    "v_cndmask_b32_dpp v111, v123, v127, vcc quad_perm:[2,3,0,1] row_mask:0xf bank_mask:0xf\n"
    "v_permlane32_swap_b32 v104, v106\n"
    "v_permlane32_swap_b32 v105, v107\n"
    "buffer_store_dwordx4 v[104:107], v246, s[36:39], 0 offen offset:192 sc1 nt\n"
    "v_permlane32_swap_b32 v108, v110\n"
    "v_permlane32_swap_b32 v109, v111\n"
    "buffer_store_dwordx4 v[108:111], v246, s[36:39], 0 offen offset:448 sc1 nt\n"
    "s_endpgm\n"
    ".Lq_fb_%=:\n"
    "s_waitcnt vmcnt(0) lgkmcnt(0)\n"
    "s_nop 15\n"
    : "+s"(ka), "+s"(bid), "+v"(tid)
    : "s"((unsigned)(size_t)(lds_ptr)lds)
    : "v4", "v5", "v6", "v7", "v8", "v9", "v10", "v11", "v12", "v13", "v14", "v15", "v16", "v17", "v18", "v19", "v20", "v21", "v22", "v23", "v24", "v25", "v26", "v27", "v28", "v29", "v30", "v31", "v32", "v33", "v34", "v35", "v36", "v37", "v38", "v39", "v40", "v41", "v42", "v43", "v44", "v45", "v46", "v47", "v48", "v49", "v50", "v51", "v52", "v53", "v54", "v55", "v56", "v57", "v58", "v59", "v60", "v61", "v62", "v63", "v64", "v65", "v66", "v67", "v68", "v69", "v70", "v71", "v72", "v73", "v74", "v75", "v76", "v77", "v78", "v79", "v80", "v81", "v82", "v83", "v84", "v85", "v86", "v87", "v88", "v89", "v90", "v91", "v92", "v93", "v94", "v95", "v96", "v97", "v98", "v99", "v100", "v101", "v102", "v103", "v104", "v105", "v106", "v107", "v108", "v109", "v110", "v111", "v112", "v113", "v114", "v115", "v116", "v117", "v118", "v119", "v120", "v121", "v122", "v123", "v124", "v125", "v126", "v127", "v128", "v129", "v130", "v131", "v132", "v133", "v134", "v135", "v136", "v137", "v138", "v139", "v140", "v141", "v142", "v143", "v144", "v145", "v146", "v147", "v148", "v149", "v150", "v151", "v152", "v153", "v154", "v155", "v156", "v157", "v158", "v159", "v160", "v161", "v162", "v163", "v164", "v165", "v166", "v167", "v168", "v169", "v170", "v171", "v172", "v173", "v174", "v175", "v176", "v177", "v178", "v179", "v180", "v181", "v182", "v183", "v184", "v185", "v186", "v187", "v188", "v189", "v190", "v191", "v192", "v193", "v194", "v195", "v196", "v197", "v198", "v199", "v200", "v201", "v202", "v203", "v204", "v205", "v206", "v207", "v208", "v209", "v210", "v211", "v212", "v213", "v214", "v215", "v216", "v217", "v218", "v219", "v220", "v221", "v222", "v223", "v224", "v225", "v226", "v227", "v228", "v229", "v230", "v231", "v232", "v233", "v234", "v235", "v236", "v237", "v238", "v239", "v240", "v241", "v242", "v243", "v244", "v245", "v246", "v247", "v248", "v249", "v250", "v251", "v252", "v253", "a0", "a1", "a2", "a3", "a4", "a5", "a6", "a7", "a8", "a9", "a10", "a11", "a12", "a13", "a14", "a15", "a16", "a17", "a18", "a19", "a20", "a21", "a22", "a23", "a24", "a25", "a26", "a27", "a28", "a29", "a30", "a31", "a32", "a33", "a34", "a35", "a36", "a37", "a38", "a39", "a40", "a41", "a42", "a43", "a44", "a45", "a46", "a47", "a48", "a49", "a50", "a51", "a52", "a53", "a54", "a55", "a56", "a57", "a58", "a59", "a60", "a61", "a62", "a63", "a64", "a65", "a66", "a67", "s8", "s9", "s10", "s11", "s12", "s13", "s14", "s15", "s16", "s17", "s18", "s19", "s20", "s21", "s22", "s23", "s24", "s25", "s26", "s27", "s28", "s29", "s30", "s31", "s34", "s35", "s36", "s37", "s38", "s39", "s40", "s41", "s42", "s43", "s44", "s45", "s46", "s47", "s48", "s49", "s50", "s51", "s52", "s53", "s54", "s55", "vcc", "scc", "memory");
  const unsigned pair = (bid & 7u) * 16u + (bid >> 4), half = (bid >> 3) & 1u, w = tid >> 6;
  const size_t row0 = (size_t)(pair * 2 + half) * 128 + w * 32;
  safe_rows(ka->Q + row0 * 128, ka->K + (size_t)pair * 65536, ka->V + (size_t)pair * 65536, ka->O + row0 * 128, tid & 63u);
}

extern "C" void kernel_launch(void* const* d_in, const int* in_sizes, int n_in, void* d_out, int out_size, void* d_ws, size_t ws_size, hipStream_t stream) {
  gqa_fwd<<<256, 256, 0, stream>>>((const unsigned short*)d_in[0], (const unsigned short*)d_in[1], (const unsigned short*)d_in[2], (unsigned short*)d_out);
}
```
